```python
import torch

def _softmax(x, axis=-1):
    return torch.softmax(x, dim=axis)
def _gelu(x):
    return torch.nn.functional.gelu(x, approximate="tanh")
def _silu(x):
    return torch.nn.functional.silu(x)
def _sigmoid(x):
    return torch.sigmoid(x)
def _layernorm(x, axis=-1, eps=1e-5):
    m = x.mean(dim=axis, keepdim=True)
    v = x.var(dim=axis, keepdim=True, unbiased=False)
    return (x - m) / torch.sqrt(v + eps)
def _scatter_add(tgt, idx, src, axis=0):
    return tgt.clone().index_add_(axis, idx, src)
def _segment_sum(src, seg, n):
    out = torch.zeros((n,) + src.shape[1:], dtype=src.dtype)
    return out.index_add_(0, seg, src)
def _pad(x, pw):
    flat = tuple(v for pair in reversed(pw) for v in pair)
    return torch.nn.functional.pad(x, flat)

kernel_name = 'parallel_sum__ssm_conv_s4d__glu_gated_glu__prompt'

def setup_inputs():

    _w = (lambda a: (lambda t: t.to(torch.bfloat16) if t.is_floating_point() else t)(torch.as_tensor(a)))
    def _A_setup():
        Bsz, L, D, N = 1, 2048, 512, 128
        return {
            "u": _w((lambda *s: torch.randn(*s, dtype=torch.bfloat16) * 0.05)(Bsz, L, D)),
            "log_A_real": _w((lambda *s: torch.randn(*s, dtype=torch.bfloat16) * 3.2)(D, N) * 0.5),
            "A_imag": _w((lambda *s: torch.randn(*s, dtype=torch.bfloat16) * 0.1)(D, N) * 3.14),
            "C_real": _w((lambda *s: torch.randn(*s, dtype=torch.bfloat16) * 0.8)(D, N) / N ** 0.5),
            "C_imag": _w((lambda *s: torch.randn(*s, dtype=torch.bfloat16) * 0.8)(D, N) / N ** 0.5),
            "log_dt": _w((lambda *s: torch.randn(*s, dtype=torch.bfloat16) * 0.1)(D) * 0.5 - 4.0),
            "D_skip": _w((lambda *s: torch.randn(*s, dtype=torch.bfloat16) * 0.1)(D)),
        }
    _A_in = _A_setup()

    def _B_setup():
        B, S, D, F = 1, 2048, 512, 1376
        return {
            "x": _w((lambda *s: torch.randn(*s, dtype=torch.bfloat16) * 0.05)(B, S, D)),
            "w_gate": _w((lambda *s: torch.randn(*s, dtype=torch.bfloat16) * 0.05)(D, F)),
            "w_up": _w((lambda *s: torch.randn(*s, dtype=torch.bfloat16) * 0.05)(D, F)),
            "w_down": _w((lambda *s: torch.randn(*s, dtype=torch.bfloat16) * 0.05)(F, D)),
        }
    _B_in = _B_setup()

    return {
        "A_u": _A_in["u"],
        "A_log_A_real": _A_in["log_A_real"],
        "A_A_imag": _A_in["A_imag"],
        "A_C_real": _A_in["C_real"],
        "A_C_imag": _A_in["C_imag"],
        "A_log_dt": _A_in["log_dt"],
        "A_D_skip": _A_in["D_skip"],
        "B_x": _B_in["x"],
        "B_w_gate": _B_in["w_gate"],
        "B_w_up": _B_in["w_up"],
        "B_w_down": _B_in["w_down"],
    }

def reference(A_u, A_log_A_real, A_A_imag, A_C_real, A_C_imag, A_log_dt, A_D_skip, B_x, B_w_gate, B_w_up, B_w_down):
    def _A_ref(u, log_A_real, A_imag, C_real, C_imag, log_dt, D_skip):
        L = u.shape[1]
        dt = torch.exp(log_dt)
        A = -torch.exp(log_A_real) + 1j * A_imag
        C = (C_real + 1j * C_imag) * dt[:, None]
        pos = (lambda x, dt: x.to(dt))(torch.arange(L), log_dt.dtype)
        P = torch.exp((A * dt[:, None])[:, :, None] * pos[None, None, :])
        K = 2.0 * torch.einsum("dn,dnl->dl", C, P).real
        L2 = 2 * L
        u_t = (lambda x, *d: x.permute(*d))(u, 0, 2, 1)
        Kf = (lambda x, *a, **k: torch.fft.rfft(x.float(), *a, **k))(K, L2)
        Uf = (lambda x, *a, **k: torch.fft.rfft(x.float(), *a, **k))(u_t, L2)
        y = (lambda x, *a, **k: torch.fft.irfft(x, *a, **k).to(torch.bfloat16))(Uf * Kf[None, :, :], L2)[:, :, :L]
        y = (lambda x, *d: x.permute(*d))(y, 0, 2, 1)
        return y + u * D_skip[None, None, :]

    def _B_ref(x, w_gate, w_up, w_down):
        g = torch.einsum("bsd,df->bsf", x, w_gate)
        u = torch.einsum("bsd,df->bsf", x, w_up)
        h = _sigmoid(g) * u
        return torch.einsum("bsf,fd->bsd", h, w_down)

    x = A_u
    xn = _layernorm(x, axis=-1)
    return x + _A_ref(xn, A_log_A_real, A_A_imag, A_C_real, A_C_imag, A_log_dt, A_D_skip) + _B_ref(xn, B_w_gate, B_w_up, B_w_down)
```

```cpp
#include <hip/hip_runtime.h>
#include <stdint.h>

#if defined(__HIP_DEVICE_COMPILE__)
#pragma clang attribute push(__attribute__((target("no-packed-fp32-ops"))), apply_to = function)
#endif
namespace {
typedef unsigned short u16;
typedef unsigned u32;
typedef unsigned long long u64;
typedef u32 v2u __attribute__((ext_vector_type(2)));
typedef u32 v4u __attribute__((ext_vector_type(4)));
typedef u32 v8u __attribute__((ext_vector_type(8)));
typedef short s16x4 __attribute__((ext_vector_type(4)));
typedef __bf16 bf16x2 __attribute__((ext_vector_type(2)));
typedef __bf16 bf16x8 __attribute__((ext_vector_type(8)));
typedef __bf16 bf16x16 __attribute__((ext_vector_type(16)));
typedef float f32x2 __attribute__((ext_vector_type(2)));
typedef float f32x4 __attribute__((ext_vector_type(4)));
typedef float f32x16 __attribute__((ext_vector_type(16)));
#define LAS __attribute__((address_space(3)))
#define DEV __device__ __forceinline__

constexpr int SEQ = 2048, DM = 512, NS = 128, FF = 1376, NFT = 86;

constexpr u32 WS_EPOCH = 0, WS_FS = 1024, WS_FX = 2048, WS_FH = 8192, WS_FZ = 16384, WS_STATS = 32768;
constexpr u32 WS_XT = 1u << 20, WS_ZT = 3u << 20, WS_H = 5u << 20, WS_END = 11u << 20, WS_DBG = 12u << 20;

constexpr u32 SLOT = 40992, S_WG = 16384, S_WU = 16384 + 12288 + 32;
constexpr u32 L_R = 3 * SLOT, R_COPY = 4224, R_CH = 4224 + 4160;
constexpr u32 L_U = L_R + 2 * R_CH, U_COPY = 4576, U_CH = 4576 + 4544;
constexpr u32 L_MISC = L_U + 2 * U_CH;
constexpr u32 L_KPART = L_U, KPART = 2576, L_PARAM = L_U + 4 * KPART;
constexpr u32 L_FLAG = L_MISC, L_ST = L_MISC + 128, L_CS = L_ST + 512, L_KT = L_CS + 512, KT_CH = 1312;
constexpr u32 L_END = L_KT + 2 * KT_CH;
constexpr u32 L_Z = L_R, L_P = 1 * SLOT, P_PITCH = 272, P_BUF = 64 * P_PITCH;
static_assert(L_END <= 163840 && L_PARAM + 4096 <= L_MISC, "LDS map");
enum { LF_E = 0, LF_CS = 4, LF_S = 8, LF_W = 10, LF_T = 12, LF_RF = 16, LF_CV = 20, LF_Z = 24, LF_P = 28 };

constexpr u32 SPIN_CAP = 1u << 18;
constexpr float LOG2E = 1.44269504088896341f, INV2PI = 0.15915494309189535f;

struct Params {
  const u16 *x, *lar, *aim, *cre, *cim, *ldt, *dsk, *wg, *wu, *wd;
  u16* out;
  char* ws;
};

DEV float u2f(u32 v) { return __builtin_bit_cast(float, v); }
DEV u32 f2u(float v) { return __builtin_bit_cast(u32, v); }
DEV int imin(int a, int b) { return a < b ? a : b; }
DEV int imax(int a, int b) { return a > b ? a : b; }
template <class T> DEV T lds_ld(u32 a) { return *(LAS const T*)(size_t)a; }
template <class T> DEV void lds_st(u32 a, T v) { *(LAS T*)(size_t)a = v; }
DEV v2u lds_ld64(u32 a) { return __builtin_bit_cast(v2u, __hip_atomic_load((LAS u64*)(size_t)a, __ATOMIC_RELAXED, __HIP_MEMORY_SCOPE_WAVEFRONT)); }
DEV v2u lds_tr(u32 a) { return __builtin_bit_cast(v2u, __builtin_amdgcn_ds_read_tr16_b64_v4i16((LAS s16x4*)(size_t)a)); }
DEV v4u cat(v2u a, v2u b) { return (v4u){a.x, a.y, b.x, b.y}; }
DEV float bflo(u32 v) { return u2f(v << 16); }
DEV float bfhi(u32 v) { return u2f(v & 0xffff0000u); }
DEV u32 pk(float lo, float hi) { return __builtin_bit_cast(u32, __builtin_convertvector((f32x2){lo, hi}, bf16x2)); }
DEV float rbf(float v) { return bflo(pk(v, v)); }
DEV f32x16 mfma32(v4u a, v4u b, f32x16 c) { return __builtin_amdgcn_mfma_f32_32x32x16_bf16(__builtin_bit_cast(bf16x8, a), __builtin_bit_cast(bf16x8, b), c, 0, 0, 0); }
DEV f32x4 mfma16(v4u a, v4u b, f32x4 c) { return __builtin_amdgcn_mfma_f32_16x16x32_bf16(__builtin_bit_cast(bf16x8, a), __builtin_bit_cast(bf16x8, b), c, 0, 0, 0); }
DEV void fence_sched() { __builtin_amdgcn_sched_barrier(0); }
DEV void bar() { fence_sched(); asm volatile("s_waitcnt lgkmcnt(0)\n\ts_barrier" ::: "memory"); fence_sched(); }
template <int N> DEV void wait_vm() { asm volatile("s_waitcnt vmcnt(%0)" ::"i"(N) : "memory"); }
DEV v4u rsrc(const void* p, u32 bytes) { u64 a = (u64)p; return (v4u){(u32)a, (u32)(a >> 32) & 0xffffu, bytes, 0x00020000u}; }
DEV void dma(u32 lds, u32 voff, v4u rs, u32 soff) {
  asm volatile("s_mov_b32 m0, %0\n\ts_nop 0\n\tbuffer_load_dwordx4 %1, %2, %3 offen lds" ::"s"(lds), "v"(voff), "s"(rs), "s"(soff) : "memory");
}
typedef __amdgpu_buffer_rsrc_t brs;
DEV v4u ld_sc1(brs r, u32 voff, u32 soff) { return __builtin_amdgcn_raw_buffer_load_b128(r, voff, soff, 16); }
DEV void st_sc1(brs r, v4u v, u32 voff, u32 soff) { __builtin_amdgcn_raw_buffer_store_b128(v, r, voff + soff, 0, 16); }
DEV void flag_set(char* ws, u32 off, u32 tag, int lane) {
  wait_vm<0>();
  if (lane == 0) __hip_atomic_store((u32*)(ws + off), tag, __ATOMIC_RELAXED, __HIP_MEMORY_SCOPE_AGENT);
}
DEV void flag_wait(char* ws, u32 lane_off, u32 tag) {
#pragma unroll 1
  for (u32 i = 0; i < SPIN_CAP; i++) {
    u32 v = __hip_atomic_load((u32*)(ws + lane_off), __ATOMIC_RELAXED, __HIP_MEMORY_SCOPE_AGENT);
    if (__builtin_amdgcn_ballot_w64(v != tag) == 0) break;
    __builtin_amdgcn_s_sleep(1);
  }
  asm volatile("" ::: "memory");
}
DEV void lf_set(int idx) {
  asm volatile("s_waitcnt lgkmcnt(0)" ::: "memory");
  __hip_atomic_store((LAS u32*)(size_t)(L_FLAG + 4 * idx), 1u, __ATOMIC_RELAXED, __HIP_MEMORY_SCOPE_WORKGROUP);
  asm volatile("" ::: "memory");
}
DEV void lf_wait(int idx, int mask, int lane) {
  u32 a = L_FLAG + 4 * (idx + (lane & mask));
#pragma unroll 1
  for (u32 i = 0; i < SPIN_CAP * 8; i++) {
    u32 v = __hip_atomic_load((LAS u32*)(size_t)a, __ATOMIC_RELAXED, __HIP_MEMORY_SCOPE_WORKGROUP);
    if (__builtin_amdgcn_ballot_w64(v == 0) == 0) break;
    __builtin_amdgcn_s_sleep(1);
  }
  asm volatile("" ::: "memory");
}

#define TS(i)
#define TS_DECL
#define TS_FLUSH

DEV void cexp(float al, float be, float pos, float& re, float& im) {
  float e = __builtin_amdgcn_exp2f(al * pos);
  float t = __builtin_amdgcn_fractf(be * pos);
  re = e * __builtin_amdgcn_cosf(t);
  im = e * __builtin_amdgcn_sinf(t);
}

DEV void kgen(const Params& p, int w, int lane) {
  const int ch = 2 * (int)__builtin_amdgcn_workgroup_id_x() + (w >> 1), n = ch * NS + (w & 1) * 64 + lane;
  float lar = bflo(p.lar[n]), aim = bflo(p.aim[n]), cre = bflo(p.cre[n]), cim = bflo(p.cim[n]);
  float dt = rbf(__builtin_amdgcn_exp2f(bflo(p.ldt[ch]) * LOG2E));
  float are = -rbf(__builtin_amdgcn_exp2f(lar * LOG2E)) * dt;
  const u32 pb = L_PARAM + w * 1024;
  lds_st<f32x4>(pb + lane * 16, (f32x4){are * LOG2E, aim * dt * INV2PI, 2.f * cre * dt, 2.f * cim * dt});
  const int i = lane & 15, q = lane >> 4;
  const float pv = 16 * i, p0 = i;
  const float p1 = i < 8 ? 256 + 2 * i : 512 + 256 * ((i >> 2) & 1) + 4 * (i & 3);
  const float p2 = i < 8 ? 1024 + 256 * (i >> 1) + 8 * (i & 1) : 2048;
  f32x4 k0 = {}, k1 = {}, k2 = {};
#pragma unroll 1
  for (int ks = 0; ks < 4; ks++) {
    v4u v, u0, u1, u2;
#pragma unroll
    for (int m = 0; m < 4; m++) {
      f32x4 a = lds_ld<f32x4>(pb + (ks * 16 + q * 4 + m) * 16);
      float re, im;
      cexp(a.x, a.y, pv, re, im); v[m] = pk(a.z * re - a.w * im, -(a.z * im + a.w * re));
      cexp(a.x, a.y, p0, re, im); u0[m] = pk(re, im);
      cexp(a.x, a.y, p1, re, im); u1[m] = pk(re, im);
      cexp(a.x, a.y, p2, re, im); u2[m] = pk(re, im);
    }
    k0 = mfma16(u0, v, k0); k1 = mfma16(u1, v, k1); k2 = mfma16(u2, v, k2);
  }
  const u32 kb = L_KPART + w * KPART;
  lds_st<f32x4>(kb + (16 * i + 4 * q) * 4, k0);
  lds_st<f32x4>(kb + (q < 2 ? 256 + 8 * i + 4 * q : 384 + 64 * (q - 2) + 4 * i) * 4, k1);
  if (q < 2) {
    lds_st<f32x2>(kb + (512 + 64 * q + 2 * i) * 4, (f32x2){k2.x, k2.y});
    lds_st<f32x2>(kb + (544 + 64 * q + 2 * i) * 4, (f32x2){k2.z, k2.w});
  } else if (lane == 32) lds_st<float>(kb + 640 * 4, k2.x);
}

template <int Y> DEV void conv_body(u32 ra, u32 ua, f32x4 (&acc)[4]) {
  constexpr int C[4] = {Y ? 1 : 0, Y ? 2 : 3, Y ? 5 : 4, Y ? 6 : 7};
  constexpr int NF = C[3] + 1;
#pragma unroll 1
  for (int e0 = 0; e0 < 8; e0++) {
    v4u t[NF], g[NF];
#pragma unroll
    for (int j = 0; j < NF; j++) {
      t[j] = cat(lds_ld64(ra + (7 - j) * 512), lds_ld64(ra + (7 - j) * 512 + 8));
      g[j] = lds_ld<v4u>(ua + j * 512);
    }
#pragma unroll
    for (int s = 0; s < NF; s++)
#pragma unroll
      for (int a = 0; a < 4; a++)
#pragma unroll
        for (int e1 = 0; e1 <= C[a]; e1++)
          if ((e1 > C[a] - e1 ? e1 : C[a] - e1) == s) acc[a] = mfma16(t[e1], g[C[a] - e1], acc[a]);
    ra -= 64; ua -= 64;
  }
}

__global__ __launch_bounds__(512, 2) void fused_kernel(Params p) {
  __shared__ __attribute__((aligned(16))) char smem[163840];
  asm volatile("" ::"v"((u32)(size_t)(LAS char*)smem));
  const int tid = (int)__builtin_amdgcn_workitem_id_x(), lane = tid & 63, w = __builtin_amdgcn_readfirstlane(tid >> 6);
  const int b = (int)__builtin_amdgcn_workgroup_id_x(), xcd = b & 7, sb = b >> 3;
  const int rt = (xcd & 1) * 8 + (sb >> 2), ct = (xcd >> 1) * 4 + (sb & 3);
  const bool freecu = ct == 15;
  const int kc0 = (rt + 2 * (ct & 3) + ((ct >> 2) & 1)) & 7;
  const int tm = 4 * xcd + (sb >> 3), tn = sb & 7;
  const int rot = (5 * tn + 11 * (tm & 3)) % 43;
  const brs wsr = __builtin_amdgcn_make_buffer_rsrc(p.ws, 0, WS_END, 0x00020000);
  const v4u rs_wd = rsrc(p.wd, FF * DM * 2);
  const u32 wd_voff = (lane >> 3) * 1024 + 16 * ((lane & 7) ^ (4 * ((lane >> 4) & 1)));
  v4u xres;
  TS_DECL;
  TS(0);

  if (w < 4) {
    __builtin_amdgcn_s_setprio(2);
    const int h = lane >> 5, l31 = lane & 31;
    u32 tag;
    if (!freecu) {
      kgen(p, w, lane);
      TS(1);
      const int wm = w & 1, wn = w >> 1;
      u32 xa[4];
#pragma unroll
      for (int ks = 0; ks < 4; ks++) xa[ks] = (wm * 64 + l31) * 128 + 16 * ((2 * ks + h) ^ ((l31 >> 1) & 7));
      const int pp = lane & 3, qq = (lane >> 2) & 3;
      const u32 wbase = S_WG + ((lane >> 4) & 1) * (S_WU - S_WG) + (8 * h + qq) * 192 + 8 * (((pp & 1) << 1) | (pp >> 1)) + wn * 96;
      u32 wa[3] = {wbase + (wm ? 64 : 0), wbase + 32, wbase + (wm ? 0 : 64)};
      const int srow = lane & 15;
      const u32 one2 = (srow < 8 && ((srow >> 1) & 1) == h) ? 0x3f803f80u : 0u;
      const v4u sel = srow < 4 ? (v4u){one2, one2, 0u, 0u} : (v4u){0u, 0u, one2, one2};
      const int sidx = (srow & 1) ? 0xEEEE : 0x4444;
      f32x16 acc[2][3] = {};
      f32x4 cs = {};
      v4u xA[2], xB[2], w2A, w2B;
      v8u wA, wB;
      auto load = [&](v4u(&xf)[2], v8u& w01, v4u& w2, u32 so, int ks) {
        v2u a0 = lds_tr(wa[0] + so + ks * 3072), a1 = lds_tr(wa[0] + so + ks * 3072 + 768);
        xf[0] = lds_ld<v4u>(xa[ks] + so);
        v2u b0 = lds_tr(wa[1] + so + ks * 3072), b1 = lds_tr(wa[1] + so + ks * 3072 + 768);
        xf[1] = lds_ld<v4u>(xa[ks] + so + 4096);
        w2 = cat(lds_tr(wa[2] + so + ks * 3072), lds_tr(wa[2] + so + ks * 3072 + 768));
        w01 = (v8u){a0.x, a0.y, a1.x, a1.y, b0.x, b0.y, b1.x, b1.y};
      };
      auto mma = [&](const v4u(&xf)[2], const v8u& w01, const v4u& w2) {
        const v4u w0 = w01.lo, w1 = w01.hi;
        acc[0][0] = mfma32(w0, xf[0], acc[0][0]);
        acc[0][1] = mfma32(w1, xf[0], acc[0][1]);
        cs = __builtin_amdgcn_smfmac_f32_16x16x64_bf16(__builtin_bit_cast(bf16x8, sel), __builtin_bit_cast(bf16x16, w01), cs, sidx, 0, 0);
        acc[1][0] = mfma32(w0, xf[1], acc[1][0]);
        acc[1][1] = mfma32(w1, xf[1], acc[1][1]);
        acc[0][2] = mfma32(w2, xf[0], acc[0][2]);
        acc[1][2] = mfma32(w2, xf[1], acc[1][2]);
      };
      auto interleave = [&]() {
#pragma unroll
        for (int i = 0; i < 4; i++) {
          __builtin_amdgcn_sched_group_barrier(0x008, 1, 0);
          __builtin_amdgcn_sched_group_barrier(0x100, 2, 0);
        }
        __builtin_amdgcn_sched_group_barrier(0x008, 3, 0);
        fence_sched();
      };
      bar();
      TS(2);
      tag = __builtin_amdgcn_readfirstlane(lds_ld<u32>(L_FLAG + 4 * LF_E));
      u32 so = 0;
      load(xA, wA, w2A, so, 0);
      fence_sched();
#pragma unroll 1
      for (int i = 0; i < 8; i++) {
        load(xB, wB, w2B, so, 1); mma(xA, wA, w2A); interleave();
        load(xA, wA, w2A, so, 2); mma(xB, wB, w2B); interleave();
        load(xB, wB, w2B, so, 3); mma(xA, wA, w2A); interleave();
        if (i < 7) bar();
        so = so == 2 * SLOT ? 0 : so + SLOT;
        load(xA, wA, w2A, so, 0); mma(xB, wB, w2B); interleave();
      }
      TS(3);
      if (h == 0) lds_st<float>(L_CS + w * 128 + l31 * 4, cs.x + cs.y);
      lf_set(LF_CS + w);
      const u32 st0 = lds_ld<u32>(L_ST + (wm * 64 + l31) * 4), st1 = lds_ld<u32>(L_ST + (wm * 64 + 32 + l31) * 4);
      lf_wait(LF_CS + (w ^ 1), 0, lane);
      const float csv[3] = {cs.x + cs.y, cs.z + cs.w, lds_ld<float>(L_CS + (w ^ 1) * 128 + l31 * 4)};
      v4u mb[2];
#pragma unroll
      for (int tb = 0; tb < 2; tb++) {
        u32 nm = h ? 0u : (((tb ? st1 : st0) & 0xffffu) ^ 0x8000u);
        mb[tb] = (v4u){nm | (nm << 16), nm, 0u, 0u};
      }
#pragma unroll
      for (int j = 0; j < 3; j++) {
        float c0 = csv[j], r1 = c0 - bfhi(f2u(c0)), r2 = r1 - bfhi(f2u(r1));
        v4u ca = {(f2u(c0) >> 16) | (f2u(r1) & 0xffff0000u), f2u(r2) >> 16, 0u, 0u};
        acc[0][j] = mfma32(ca, mb[0], acc[0][j]);
        acc[1][j] = mfma32(ca, mb[1], acc[1][j]);
      }
#pragma unroll
      for (int j = 0; j < 3; j++) {
        const int ftg = 6 * ct + 3 * wn + (wm ? 2 - j : j);
        if (ftg < NFT) {
#pragma unroll
          for (int tb = 0; tb < 2; tb++) {
            const float sd = bfhi(tb ? st1 : st0), a = -LOG2E * __builtin_amdgcn_rcpf(sd);
            float hv[8];
#pragma unroll
            for (int r = 0; r < 8; r++) {
              float e = __builtin_amdgcn_exp2f(acc[tb][j][r] * a);
              hv[r] = acc[tb][j][8 + r] * __builtin_amdgcn_rcpf(__builtin_fmaf(e, sd, sd));
            }
            v4u o = {pk(hv[0], hv[1]), pk(hv[2], hv[3]), pk(hv[4], hv[5]), pk(hv[6], hv[7])};
            st_sc1(wsr, o, lane * 16, WS_H + ((4 * rt + 2 * wm + tb) * NFT + ftg) * 1024);
          }
        }
      }
      TS(4);
      flag_set(p.ws, WS_FH + ((2 * rt + wm) * 32 + 2 * ct + wn) * 4, tag, lane);
      TS(5);
    } else {
      u32 xa[4];
#pragma unroll
      for (int ks = 0; ks < 4; ks++) xa[ks] = (w * 32 + l31) * 128 + 16 * ((2 * ks + h) ^ ((l31 >> 1) & 7));
      f32x16 gram = {}, sum = {};
      const v4u ones = {0x3f803f80u, 0x3f803f80u, 0x3f803f80u, 0x3f803f80u};
      u32 so = 0;
#pragma unroll 1
      for (int i = 0; i < 8; i++) {
        bar();
#pragma unroll
        for (int ks = 0; ks < 4; ks++) {
          v4u f = lds_ld<v4u>(xa[ks] + so);
          gram = mfma32(f, f, gram);
          sum = mfma32(ones, f, sum);
        }
        so = so == 2 * SLOT ? 0 : so + SLOT;
      }
      tag = __builtin_amdgcn_readfirstlane(lds_ld<u32>(L_FLAG + 4 * LF_E));
      lf_set(LF_CS + w);
      const int dr = (l31 & 3) + 4 * (l31 >> 3);
      float sq = gram[0];
#pragma unroll
      for (int r = 1; r < 16; r++) sq = dr == r ? gram[r] : sq;
      const float mean = sum[0] * (1.f / DM);
      const float var = rbf(__builtin_fmaxf(sq * (1.f / DM) - mean * mean, 0.f));
      const float sd = __builtin_sqrtf(rbf(var + 1e-5f));
      if (h == ((l31 >> 2) & 1)) __builtin_amdgcn_raw_buffer_store_b32(pk(mean, sd), wsr, l31 * 4, WS_STATS + (rt * 128 + w * 32) * 4, 16);
      flag_set(p.ws, WS_FS + (rt * 4 + w) * 4, tag, lane);
      TS(3);
      kgen(p, w, lane);
      bar();
      TS(5);
    }

    const int tb = w & 1, kh = w >> 1;
    const int qq = (lane >> 2) & 3;
    const u32 db = (8 * h + qq) * 128 + 32 * ((lane >> 4) & 1) + 8 * (lane & 3) + kh * 20480;
    const u32 da[2] = {db + 64 * (qq >> 1), db + 64 * (1 - (qq >> 1))};
    auto sloc = [](int i) -> u32 { i = i >= 30 ? i - 30 : i; return (u32)(((i / 10 + 2) % 3) * SLOT + (i % 10) * 2048); };
    auto wfrag = [&](int i, int dt) { return cat(lds_tr(da[dt] + sloc(i)), lds_tr(da[dt] + sloc(i) + 512)); };
    xres = *(const v4u*)(p.x + (size_t)(64 * tm + (tid >> 3)) * DM + 64 * tn + 8 * (tid & 7));
    lf_wait(LF_W + kh, 0, lane);
    v4u wr[13][2];
#pragma unroll
    for (int i = 0; i < 13; i++) { wr[i][0] = wfrag(i, 0); wr[i][1] = wfrag(i, 1); }
    lf_set(LF_T + w);
    lf_wait(LF_T + (w ^ 1), 0, lane);
    {
      int ph = rot + 30; ph = ph >= 43 ? ph - 43 : ph;
#pragma unroll
      for (int i = 30; i < 43; i++) {
        dma(sloc(i) + kh * 20480 + tb * 1024, wd_voff, rs_wd, ((43 * kh + ph) * 16 + 8 * tb) * 1024 + tn * 128);
        ph = ph == 42 ? 0 : ph + 1;
      }
    }
    TS(6);
    flag_wait(p.ws, WS_FH + (tm * 32 + 14 * kh + (lane < 15 ? lane : 0)) * 4, tag);
    lf_set(LF_RF + w);
    TS(7);
    constexpr int PF = 10;
    v4u hb[PF];
    const u32 hbase = WS_H + ((2 * tm + tb) * NFT + 43 * kh) * 1024;
    int ph = rot;
#pragma unroll
    for (int i = 0; i < PF; i++) { hb[i] = ld_sc1(wsr, lane * 16, hbase + ph * 1024); ph = ph == 42 ? 0 : ph + 1; }
    fence_sched();
    f32x16 o0 = {}, o1 = {};
    v4u n0, n1;
#pragma unroll
    for (int i = 0; i < 43; i++) {
      v4u c0 = i < 13 ? wr[i < 13 ? i : 0][0] : n0, c1 = i < 13 ? wr[i < 13 ? i : 0][1] : n1;
      if (i == 29) { lf_set(LF_P + w); lf_wait(LF_RF + (w ^ 1), 0, lane); }
      if (i >= 12 && i < 42) { n0 = wfrag(i + 1, 0); n1 = wfrag(i + 1, 1); }
      o0 = mfma32(c0, hb[i % PF], o0);
      o1 = mfma32(c1, hb[i % PF], o1);
      if (i + PF < 43) { hb[i % PF] = ld_sc1(wsr, lane * 16, hbase + ph * 1024); ph = ph == 42 ? 0 : ph + 1; }
      fence_sched();
    }
    TS(8);
    lf_wait(LF_Z, 7, lane);
    TS(9);
    {
      const int row = 32 * kh + l31;
#pragma unroll
      for (int ks = 0; ks < 2; ks++) {
        v4u zf = lds_ld<v4u>(L_Z + row * 128 + 16 * ((4 * tb + 2 * ks + h) ^ ((row >> 1) & 7)));
        const int dlt = l31 - 16 * ks - 8 * h;
        v4u id;
#pragma unroll
        for (int j = 0; j < 4; j++) id[j] = dlt == 2 * j ? 0x3f80u : dlt == 2 * j + 1 ? 0x3f800000u : 0u;
        if (kh) o1 = mfma32(zf, id, o1); else o0 = mfma32(zf, id, o0);
      }
      const u32 pa = L_P + kh * P_BUF + (32 * tb + l31) * P_PITCH + 16 * h;
#pragma unroll
      for (int g = 0; g < 4; g++) {
        lds_st<f32x4>(pa + 32 * g, (f32x4){o0[4 * g], o0[4 * g + 1], o0[4 * g + 2], o0[4 * g + 3]});
        lds_st<f32x4>(pa + 128 + 32 * g, (f32x4){o1[4 * g], o1[4 * g + 1], o1[4 * g + 2], o1[4 * g + 3]});
      }
    }
  } else {
    u32 tag;
    if (w < 6) {
      __builtin_amdgcn_s_setprio(3);
      const int kh = w - 4;
      if (w == 4) {
        const v4u rs = rsrc(p.x, SEQ * DM * 2);
        const u32 v0 = (lane >> 3) * 1024 + 16 * ((lane & 7) ^ (lane >> 4)), v1 = v0 ^ 64;
        auto issue = [&](int c, int p0, int p1) {
          const u32 so = rt * 131072 + ((kc0 + c) & 7) * 128, lb = (c % 3) * SLOT;
#pragma unroll
          for (int pi = p0; pi < p1; pi++) dma(lb + pi * 1024, (pi & 1) ? v1 : v0, rs, so + pi * 8192);
        };
        constexpr int H = 10;
        issue(0, 0, 16); issue(1, 0, H);
#pragma unroll 1
        for (int i = 0; i < 6; i++) { wait_vm<H>(); bar(); issue(i + 1, H, 16); issue(i + 2, 0, H); }
        wait_vm<H>(); bar(); issue(7, H, 16);
      } else {
        const v4u rg = rsrc(p.wg, DM * FF * 2), ru = rsrc(p.wu, DM * FF * 2);
        u32 vo[3];
#pragma unroll
        for (int j = 0; j < 3; j++) {
          const int u = 64 * j + lane, k = u / 12, c = u - 12 * k;
          vo[j] = (96 * ct + 8 * c < FF) ? k * (FF * 2) + 16 * c : 0x80000000u;
        }
        auto issue = [&](int c, int p0, int p1) {
          if (freecu) return;
          const u32 so = ((kc0 + c) & 7) * (64 * FF * 2) + ct * 192, lb = (c % 3) * SLOT;
#pragma unroll
          for (int pi = p0; pi < p1; pi++) {
            const int m = pi & 1, P = pi >> 1;
            dma(lb + (m ? S_WU : S_WG) + P * 1024, vo[P % 3], m ? ru : rg, so + (P / 3) * (16 * FF * 2));
          }
        };
        constexpr int H = 14;
        issue(0, 0, 24); issue(1, 0, H);
#pragma unroll 1
        for (int i = 0; i < 6; i++) { wait_vm<H>(); bar(); issue(i + 1, H, 24); issue(i + 2, 0, H); }
        wait_vm<H>(); bar(); issue(7, H, 24);
      }
      TS(1);
      int ph = rot;
      auto wd_issue = [&](int i0, int i1) {
#pragma unroll 1
        for (int i = i0; i < i1; i++) {
          const int r = i / 10;
          const u32 lb = (r == 0 ? 2 * SLOT : r == 1 ? 0 : SLOT) + kh * 20480 + (i - 10 * r) * 2048;
          const u32 so = (43 * kh + ph) * 16384 + tn * 128;
          dma(lb, wd_voff, rs_wd, so);
          dma(lb + 1024, wd_voff, rs_wd, so + 8192);
          ph = ph == 42 ? 0 : ph + 1;
        }
      };
      wd_issue(0, 5);
      wait_vm<10>(); bar();
      TS(2);
      tag = __builtin_amdgcn_readfirstlane(lds_ld<u32>(L_FLAG + 4 * LF_E));
      wd_issue(5, 20);
      lf_wait(LF_CS, 3, lane);
      wd_issue(20, 30);
      TS(3);
      wait_vm<0>();
      lf_set(LF_W + kh);
      if (freecu) bar();
      __builtin_amdgcn_s_setprio(0);
      TS(4);
    } else {
      const int sw = w - 6, ch = 2 * b + sw;
      if (sw == 0) {
        u32 e = __hip_atomic_load((u32*)(p.ws + WS_EPOCH), __ATOMIC_RELAXED, __HIP_MEMORY_SCOPE_AGENT);
        if (lane < 32) lds_st<u32>(L_FLAG + 4 * lane, lane == LF_E ? e + 1 : 0u);
      }
      const float dskip = bflo(p.dsk[ch]);
      bar();
      TS(1);
      tag = __builtin_amdgcn_readfirstlane(lds_ld<u32>(L_FLAG + 4 * LF_E));
      const int sig = 2 * kc0 + (ct >> 3);
      {
        const int tok = 64 * sw + 32 * (lane >> 5) + 4 * (lane & 7), pl = (lane >> 3) & 3;
#pragma unroll
        for (int j = 0; j < 4; j++) {
          v4u v;
#pragma unroll
          for (int i = 0; i < 4; i++) v[i] = lds_ld<u32>((tok + i) * 128 + 16 * ((4 * (ct >> 3) + j) ^ (((tok + i) >> 1) & 7)) + 4 * pl);
          st_sc1(wsr, v, (pl * SEQ + tok) * 4, WS_XT + ((sig * 16 + 4 * j) * SEQ + rt * 128) * 4);
        }
      }
      if (freecu) {
        bar();
        flag_set(p.ws, WS_FX + (sig * 32 + rt * 2 + sw) * 4, tag, lane);
        for (int i = 0; i < 7; i++) bar();
      }
      const u32 kt = L_KT + sw * KT_CH + 16;
#pragma unroll
      for (int i = 0; i < 3; i++) {
        const int e = 64 * i + lane;
        if (e < 161) {
          f32x4 s = lds_ld<f32x4>(L_KPART + 2 * sw * KPART + e * 16) + lds_ld<f32x4>(L_KPART + (2 * sw + 1) * KPART + e * 16);
          if (e == 0) s.x += dskip;
          lds_st<v2u>(kt + e * 8, (v2u){pk(s.x, s.y), pk(s.z, s.w)});
        }
      }
      const u32 rb = L_R + sw * R_CH;
      if (lane == 0) lds_st<u32>(kt - 4, 0u);
      if (lane < 8) lds_st<v4u>(rb + (lane >> 2) * R_COPY + 4096 + (lane & 3) * 16, (v4u){0u, 0u, 0u, 0u});
#pragma unroll 1
      for (int it = 0; it < 4; it++) {
        if (!freecu) {
          bar();
          if (it == 1) flag_set(p.ws, WS_FX + (sig * 32 + rt * 2 + sw) * 4, tag, lane);
        }
        const int v = 64 * it + lane, l0 = 2039 - 8 * v;
        u32 kv[9];
#pragma unroll
        for (int j = 0; j < 9; j++) {
          const int g = ((int)pk(0.f, (float)(l0 + j)) >> 16) - 0x4280;
          const int idx = imax(imin(l0 + j, g), imin(imax(l0 + j, g), 128));
          kv[j] = lds_ld<u16>(kt + 2 * idx);
        }
        lds_st<v4u>(rb + 16 * v, (v4u){kv[7] | kv[6] << 16, kv[5] | kv[4] << 16, kv[3] | kv[2] << 16, kv[1] | kv[0] << 16});
        lds_st<v4u>(rb + R_COPY + 16 * v, (v4u){kv[8] | kv[7] << 16, kv[6] | kv[5] << 16, kv[4] | kv[3] << 16, kv[2] | kv[1] << 16});
      }
      if (!freecu) bar(); else { lf_set(LF_E + 2 + sw); lf_wait(LF_E + 2, 1, lane); }
      TS(2);
      flag_wait(p.ws, lane < 32 ? WS_FS + (32 * sw + lane) * 4 : WS_FX + ((b >> 4) * 32 + 16 * sw + (lane & 15)) * 4, tag);
      TS(3);
      v4u xv[4], sv[4];
#pragma unroll
      for (int i = 0; i < 4; i++) sv[i] = ld_sc1(wsr, lane * 16, WS_STATS + (1024 * sw + 256 * i) * 4);
#pragma unroll
      for (int i = 0; i < 4; i++) xv[i] = ld_sc1(wsr, lane * 16, WS_XT + (b * SEQ + 1024 * sw + 256 * i) * 4);
      if (sw == 0 && lane < 57) {
        const u32 za = L_U + (lane < 29 ? lane * 16 : U_COPY + (lane - 29) * 16);
        lds_st<v4u>(za, (v4u){0u, 0u, 0u, 0u});
        lds_st<v4u>(za + U_CH, (v4u){0u, 0u, 0u, 0u});
      }
      if (!freecu) {
        bar();
#pragma unroll
        for (int i = 0; i < 4; i++)
          if (8 * sw + 2 * i + (lane >> 5) == rt) lds_st<v4u>(L_ST + (lane & 31) * 16, sv[i]);
        bar();
      }
      TS(4);
#pragma unroll
      for (int i = 0; i < 4; i++) {
        float a[4], c[4];
#pragma unroll
        for (int j = 0; j < 4; j++) {
          const float m = bflo(sv[i][j]), r = __builtin_amdgcn_rcpf(bfhi(sv[i][j]));
          const u32 d = pk(bflo(xv[i][j]) - m, bfhi(xv[i][j]) - m);
          a[j] = bflo(d) * r; c[j] = bfhi(d) * r;
        }
        const u32 ub = L_U + 448 + (1024 * sw + 256 * i + 4 * lane) * 2;
        const v2u pa = {pk(a[0], a[1]), pk(a[2], a[3])}, pc = {pk(c[0], c[1]), pk(c[2], c[3])};
        lds_st<v2u>(ub + U_COPY, pa); lds_st<u32>(ub + 4, pa.x); lds_st<u32>(ub + 8, pa.y);
        lds_st<v2u>(ub + U_CH + U_COPY, pc); lds_st<u32>(ub + U_CH + 4, pc.x); lds_st<u32>(ub + U_CH + 8, pc.y);
      }
      lf_set(LF_S + sw);
    }

    const int hw = w - 4, cc = hw & 1, y = hw >> 1;
    xres = *(const v4u*)(p.x + (size_t)(64 * tm + (tid >> 3)) * DM + 64 * tn + 8 * (tid & 7));
    lf_wait(LF_S, 1, lane);
    TS(5);
    const int n = lane & 15, q = lane >> 4, odd = lane & 1;
    const u32 ra = L_R + cc * R_CH + odd * R_COPY + (2044 - 4 * (n >> 1) + 8 * q) * 2 - 7 * 512;
    const u32 ua = L_U + cc * U_CH + odd * U_COPY + 448 + 64 * (n >> 1) + 16 * q;
    f32x4 acc[4] = {};
    if (y) conv_body<1>(ra, ua, acc); else conv_body<0>(ra, ua, acc);
    lf_set(LF_CV + hw);
    TS(6);
#pragma unroll
    for (int s = 0; s < 2; s++) {
      const f32x4 A = acc[2 * s], B = acc[2 * s + 1];
      const u32 a0 = pk(A.x, A.y), a1 = pk(A.z, A.w), b0 = pk(B.x, B.y), b1 = pk(B.z, B.w);
      const u32 r0 = __builtin_amdgcn_mov_dpp(odd ? a0 : b0, 0xB1, 0xF, 0xF, true), r1 = __builtin_amdgcn_mov_dpp(odd ? a1 : b1, 0xB1, 0xF, 0xF, true);
      const v4u o = odd ? (v4u){r0, b0, r1, b1} : (v4u){a0, r0, a1, r1};
      const int c = 4 * s + (odd ? 3 - y : y);
      st_sc1(wsr, o, (256 * c + 32 * (n >> 1) + 8 * q) * 2, WS_ZT + (2 * b + cc) * SEQ * 2);
    }
    flag_set(p.ws, WS_FZ + (y * DM + 2 * b + cc) * 4, tag, lane);
    TS(7);
    const int cz = (tm >> 2) & 3, yz = cz == 1 || cz == 2;
    flag_wait(p.ws, WS_FZ + (yz * DM + 64 * tn + 16 * hw + n) * 4, tag);
    TS(8);
    v4u zv[2];
#pragma unroll
    for (int i = 0; i < 2; i++) zv[i] = ld_sc1(wsr, (lane >> 3) * (SEQ * 2) + (lane & 7) * 16, WS_ZT + ((64 * tn + 16 * hw + 8 * i) * SEQ + 64 * tm) * 2);
    lf_wait(LF_CV, 3, lane);
#pragma unroll
    for (int i = 0; i < 2; i++) {
      const int row = 16 * hw + 8 * i + (lane >> 3);
      lds_st<v4u>(L_Z + row * 128 + 16 * ((lane & 7) ^ ((row >> 1) & 7)), zv[i]);
    }
    lf_set(LF_Z + hw);
    TS(9);
    if (b == 0 && w == 7) {
#pragma unroll 1
      for (int i = 0; i < 8; i++) flag_wait(p.ws, WS_FX + (64 * i + lane) * 4, tag);
      if (lane == 0) __hip_atomic_store((u32*)(p.ws + WS_EPOCH), tag, __ATOMIC_RELAXED, __HIP_MEMORY_SCOPE_AGENT);
    }
  }

  bar();
  TS(10);
  {
    const u32 pa = L_P + (tid >> 3) * P_PITCH + (tid & 7) * 32;
    const f32x4 s0 = lds_ld<f32x4>(pa) + lds_ld<f32x4>(pa + P_BUF), s1 = lds_ld<f32x4>(pa + 16) + lds_ld<f32x4>(pa + P_BUF + 16);
    const v4u o = {pk(s0.x + bflo(xres.x), s0.y + bfhi(xres.x)), pk(s0.z + bflo(xres.y), s0.w + bfhi(xres.y)),
                   pk(s1.x + bflo(xres.z), s1.y + bfhi(xres.z)), pk(s1.z + bflo(xres.w), s1.w + bfhi(xres.w))};
    *(v4u*)(p.out + (size_t)(64 * tm + (tid >> 3)) * DM + 64 * tn + 8 * (tid & 7)) = o;
  }
  TS(11);
  TS_FLUSH;
}
}
#if defined(__HIP_DEVICE_COMPILE__)
#pragma clang attribute pop
#endif

extern "C" void kernel_launch(void* const* d_in, const int* in_sizes, int n_in, void* d_out, int out_size, void* d_ws,
                              size_t ws_size, hipStream_t stream) {
  Params p;
  p.x = (const u16*)d_in[0]; p.lar = (const u16*)d_in[1]; p.aim = (const u16*)d_in[2]; p.cre = (const u16*)d_in[3];
  p.cim = (const u16*)d_in[4]; p.ldt = (const u16*)d_in[5]; p.dsk = (const u16*)d_in[6];
  p.wg = (const u16*)d_in[8]; p.wu = (const u16*)d_in[9]; p.wd = (const u16*)d_in[10];
  p.out = (u16*)d_out; p.ws = (char*)d_ws;
  fused_kernel<<<256, 512, 0, stream>>>(p);
}
```

```cpp
#include <hip/hip_runtime.h>
#include <stdint.h>

namespace {
typedef unsigned short u16;
typedef unsigned u32;
typedef unsigned long long u64;
typedef u32 v2u __attribute__((ext_vector_type(2)));
typedef u32 v4u __attribute__((ext_vector_type(4)));
typedef u64 v4q __attribute__((ext_vector_type(4)));
typedef u64 v8q __attribute__((ext_vector_type(8)));
typedef short s16x4 __attribute__((ext_vector_type(4)));
typedef __bf16 bf16x2 __attribute__((ext_vector_type(2)));
typedef __bf16 bf16x8 __attribute__((ext_vector_type(8)));
typedef float f32x2 __attribute__((ext_vector_type(2)));
typedef float f32x4 __attribute__((ext_vector_type(4)));
typedef float f32x16 __attribute__((ext_vector_type(16)));
#define LAS __attribute__((address_space(3)))
#define DEV __device__ __forceinline__

constexpr int SEQ = 2048, DM = 512, FF = 1376, NFT = 86;

constexpr u32 WS_EPOCH = 0, WS_FX = 2048, WS_STATS = 32768, WS_TSTAT = 49152, WS_FH = 131072, WS_FZ = 262144;
constexpr u32 WS_XT = 1u << 20, WS_ZT = 3u << 20, WS_H = 5u << 20, WS_END = 11u << 20;

constexpr u32 SLOT = 40960, S_WG = 16384, S_WU = 16384 + 12288;
constexpr u32 L_R = 3 * SLOT, R_COPY = 4224, R_CH = 4224 + 4160;
constexpr u32 L_U = L_R + 2 * R_CH, U_COPY = 4576, U_CH = 4576 + 4544;
constexpr u32 L_MISC = L_U + 2 * U_CH;
constexpr u32 L_KG = 2 * SLOT, KG_PITCH = 272, KG_W = 57 * KG_PITCH, KPART = 2576, L_KPART = L_MISC - 4 * KPART;
constexpr u32 L_FLAG = L_MISC, L_CS = L_FLAG + 128, L_ST = L_CS + 2048;
constexpr u32 L_SINK = L_ST + 512, L_END = L_SINK + 512;
constexpr u32 L_Z = 36 * 4096, L_P = 3 * 4096, P_PITCH = 272, P_BUF = 64 * P_PITCH, L_ZT = L_P + 2 * P_BUF, Z_PITCH = 144;
static_assert(L_END <= 163840 && L_KG + 3 * KG_W + 64 * KG_PITCH <= L_KPART && L_Z + 8192 <= L_MISC, "LDS map");
enum { LF_C = 0, LF_S = 4, LF_W = 8, LF_X = 12 };

constexpr u32 CODE_BYTES = 24320;
constexpr u32 SPIN_CAP = 1u << 15;
constexpr float LOG2E = 1.44269504088896341f, INV2PI = 0.15915494309189535f;

struct Params {
  const u16 *x, *lar, *aim, *cre, *cim, *ldt, *dsk, *wg, *wu, *wd;
  u16* out;
  char* ws;
};

DEV float u2f(u32 v) { return __builtin_bit_cast(float, v); }
DEV u32 f2u(float v) { return __builtin_bit_cast(u32, v); }
DEV int imax(int a, int b) { return a > b ? a : b; }
template <class T> DEV T lds_ld(u32 a) { return *(LAS const T*)(size_t)a; }
template <class T> DEV void lds_st(u32 a, T v) { *(LAS T*)(size_t)a = v; }
DEV v2u lds_ld64(u32 a) { return __builtin_bit_cast(v2u, __hip_atomic_load((LAS u64*)(size_t)a, __ATOMIC_RELAXED, __HIP_MEMORY_SCOPE_WAVEFRONT)); }
DEV v2u lds_tr(u32 a) { return __builtin_bit_cast(v2u, __builtin_amdgcn_ds_read_tr16_b64_v4i16((LAS s16x4*)(size_t)a)); }
DEV v4u cat(v2u a, v2u b) { return (v4u){a.x, a.y, b.x, b.y}; }
DEV float bflo(u32 v) { return u2f(v << 16); }
DEV float bfhi(u32 v) { return u2f(v & 0xffff0000u); }
DEV u32 pk(float lo, float hi) { return __builtin_bit_cast(u32, __builtin_convertvector((f32x2){lo, hi}, bf16x2)); }
DEV float rbf(float v) { return bflo(pk(v, v)); }
DEV f32x16 mfma32(v4u a, v4u b, f32x16 c) { return __builtin_amdgcn_mfma_f32_32x32x16_bf16(__builtin_bit_cast(bf16x8, a), __builtin_bit_cast(bf16x8, b), c, 0, 0, 0); }
DEV void fence_sched() { __builtin_amdgcn_sched_barrier(0); }
DEV void bar() { fence_sched(); asm volatile("s_waitcnt lgkmcnt(0)\n\ts_barrier" ::: "memory"); fence_sched(); }
template <int N> DEV void wait_vm() { asm volatile("s_waitcnt vmcnt(%0)" ::"i"(N) : "memory"); }
DEV v4u rsrc(u64 a, u32 bytes) { return (v4u){(u32)a, (u32)(a >> 32) & 0xffffu, bytes, 0x00020000u}; }
DEV void bar_raw() { fence_sched(); asm volatile("s_barrier" ::: "memory"); fence_sched(); }
DEV void dma3(u32 lds, const u32 (&v)[3], v4u rs, u32 so) {
  asm volatile("s_mov_b32 m0, %0\n\ts_nop 0\n\tbuffer_load_dwordx4 %1, %4, %5 offen lds\n\tbuffer_load_dwordx4 %2, %4, %5 offen offset:1024 lds\n\t"
               "buffer_load_dwordx4 %3, %4, %5 offen offset:2048 lds" ::"s"(lds), "v"(v[0]), "v"(v[1]), "v"(v[2]), "s"(rs), "s"(so) : "memory");
}
DEV void dma4(u32 lds, u32 v0, u32 v1, v4u rs, u32 s0, u32 s1) {
  asm volatile("s_mov_b32 m0, %0\n\ts_nop 0\n\tbuffer_load_dwordx4 %1, %3, %4 offen lds\n\tbuffer_load_dwordx4 %2, %3, %4 offen offset:1024 lds\n\t"
               "buffer_load_dwordx4 %1, %3, %5 offen offset:2048 lds\n\tbuffer_load_dwordx4 %2, %3, %5 offen offset:3072 lds"
               ::"s"(lds), "v"(v0), "v"(v1), "s"(rs), "s"(s0), "s"(s1) : "memory");
}
DEV void dma1(u32 lds, u32 v, v4u rs, u32 so) {
  asm volatile("s_mov_b32 m0, %0\n\ts_nop 0\n\tbuffer_load_dwordx4 %1, %2, %3 offen lds" ::"s"(lds), "v"(v), "s"(rs), "s"(so) : "memory");
}
DEV void touch(u32 v, v4u rs) {
  asm volatile("s_mov_b32 m0, %0\n\ts_nop 0\n\tbuffer_load_dword %1, %2, 0 offen lds" ::"s"(L_SINK), "v"(v), "s"(rs) : "memory");
}
DEV v4u ald(u32 v, v4u rs, u32 so) {
  v4u r;
  asm volatile("buffer_load_dwordx4 %0, %1, %2, %3 offen sc1" : "=v"(r) : "v"(v), "s"(rs), "s"(so) : "memory");
  return r;
}
typedef __amdgpu_buffer_rsrc_t brs;
DEV v4u ld_sc1(brs r, u32 voff, u32 soff) { return __builtin_amdgcn_raw_buffer_load_b128(r, voff, soff, 16); }
DEV void st_sc1(brs r, v4u v, u32 voff, u32 soff) { __builtin_amdgcn_raw_buffer_store_b128(v, r, voff + soff, 0, 16); }
template <int N, int W = 0> DEV void flag_set(brs r, u32 off, u32 tag, int lane) {
  wait_vm<N>();
  if (W) { if (lane < W) __builtin_amdgcn_raw_buffer_store_b128((v4u)tag, r, lane * 16, off, 16); }
  else if (lane == 0) __builtin_amdgcn_raw_buffer_store_b32(tag, r, 0, off, 16);
  asm volatile("" ::: "memory");
}
DEV void flag_wait(v4u rs, u32 voff, u32 tag) {
#pragma unroll 1
  for (u32 i = 0; i < SPIN_CAP; i++) {
    u32 v;
    asm volatile("buffer_load_dword %0, %1, %2, 0 offen sc1\n\ts_waitcnt vmcnt(0)" : "=v"(v) : "v"(voff), "s"(rs) : "memory");
    if (__builtin_amdgcn_ballot_w64(v != tag) == 0) break;
    __builtin_amdgcn_s_sleep(1);
  }
}
DEV void lf_set(int idx, u32 lvl) {
  asm volatile("s_waitcnt lgkmcnt(0)" ::: "memory");
  __hip_atomic_store((LAS u32*)(size_t)(L_FLAG + 4 * idx), lvl, __ATOMIC_RELAXED, __HIP_MEMORY_SCOPE_WORKGROUP);
  asm volatile("" ::: "memory");
}
DEV void lf_post(int idx, u32 lvl) {
  asm volatile("" ::: "memory");
  __hip_atomic_store((LAS u32*)(size_t)(L_FLAG + 4 * idx), lvl, __ATOMIC_RELAXED, __HIP_MEMORY_SCOPE_WORKGROUP);
  asm volatile("" ::: "memory");
}
DEV void lf_wait(u32 lane_idx, u32 lvl) {
  const u32 a = L_FLAG + 4 * lane_idx;
#pragma unroll 1
  for (u32 i = 0; i < SPIN_CAP * 8; i++) {
    u32 v = __hip_atomic_load((LAS u32*)(size_t)a, __ATOMIC_RELAXED, __HIP_MEMORY_SCOPE_WORKGROUP);
    if (__builtin_amdgcn_ballot_w64(v < lvl) == 0) break;
    __builtin_amdgcn_s_sleep(1);
  }
  asm volatile("" ::: "memory");
}
DEV u32 perm(u32 hi, u32 lo, u32 sel) { return __builtin_amdgcn_perm(hi, lo, sel); }
DEV float dot2(u32 a, u32 b, float c) { return __builtin_amdgcn_fdot2_f32_bf16(__builtin_bit_cast(bf16x2, a), __builtin_bit_cast(bf16x2, b), c, false); }
DEV float fold32(float a, float b) { v2u s = __builtin_amdgcn_permlane32_swap(f2u(a), f2u(b), false, false); return u2f(s.x) + u2f(s.y); }
DEV float fold16(float a, float b) { v2u s = __builtin_amdgcn_permlane16_swap(f2u(a), f2u(b), false, false); return u2f(s.x) + u2f(s.y); }
template <int C> DEV float dpp_add(float a) { return a + u2f(__builtin_amdgcn_mov_dpp(f2u(a), C, 0xF, 0xF, true)); }
DEV u32 lane_id() { u32 l; asm volatile("v_mbcnt_lo_u32_b32 %0, -1, 0\n\tv_mbcnt_hi_u32_b32 %0, -1, %0" : "=v"(l)); return l; }
DEV float row_sum(float a) { return dpp_add<0xB1>(dpp_add<0x4E>(dpp_add<0x141>(dpp_add<0x140>(a)))); }

DEV f32x2 cexp(float al, float be, float pos) {
  const float t = __builtin_amdgcn_fractf(be * pos);
  return (f32x2){__builtin_amdgcn_cosf(t), __builtin_amdgcn_sinf(t)} * __builtin_amdgcn_exp2f(al * pos);
}

constexpr int HXG = 2;
constexpr int WDF = 5;
constexpr int PF = 10, PL = 3;
constexpr int ZG = 19;

__global__ __launch_bounds__(512, 2) void fused_kernel(Params) {
  __shared__ __attribute__((aligned(16))) char smem[163840];
  v8q ka;
  v4q kb;
  asm volatile("s_load_dwordx16 %0, %2, 0x0\n\ts_load_dwordx8 %1, %2, 0x40"
               : "=&s"(ka), "=&s"(kb) : "s"(__builtin_amdgcn_kernarg_segment_ptr()), "v"((u32)(size_t)(LAS char*)smem));
  const int tid = (int)__builtin_amdgcn_workitem_id_x(), lane = tid & 63, w = __builtin_amdgcn_readfirstlane(tid >> 6);
  const int b = (int)__builtin_amdgcn_workgroup_id_x(), xcd = b & 7, sb = b >> 3;
  touch((u32)(sb + 32 * tid) * 128, rsrc(__builtin_amdgcn_s_getpc() & ~255ull, CODE_BYTES));
  const int rt = (xcd & 1) * 8 + (sb >> 2), ct = (xcd >> 1) * 4 + (sb & 3);
  const int kc0 = (rt + 2 * (ct & 3) + ((ct >> 2) & 1)) & 7;
  u32 vx0 = (lane >> 3) * 1024 + 16 * ((lane & 7) ^ (lane >> 4)), vx1 = (vx0 ^ 64) + 7168;
  u32 vo[3];
#pragma unroll
  for (int j = 0; j < 3; j++) {
    const int u = 64 * j + lane, k = (u * 5462) >> 16, c = (u - 12 * k) ^ ((k >> 2) & 2);
    vo[j] = (k * (FF * 2) + 16 * c - 1024 * j) | ((FF / 8 - 1 - 12 * ct - c) & 0x80000000u);
  }
  u32 vg = (lane << (w < 4 ? 1 : 4)) + (w < 4 ? b * 512 + w * 128 : (rt * 128 + ct * 8 + 4 * (w - 6)) * 1024);
  u32 sw0 = kc0 * (64 * FF * 2) + ct * 192, sx0 = rt * 131072 + kc0 * 128, b4 = b * 4;
  asm volatile("s_waitcnt lgkmcnt(0)" : "+s"(ka), "+s"(kb), "+v"(vx0), "+v"(vx1), "+v"(vo[0]), "+v"(vo[1]), "+v"(vo[2]), "+v"(vg),
               "+s"(sw0), "+s"(sx0), "+s"(b4));
  const u64 p_ws = kb[3];
  const v4u rs_x = rsrc(ka[0], SEQ * DM * 2), rs_g = rsrc(ka[7], DM * FF * 2), rs_u = rsrc(kb[0], DM * FF * 2);
  u32 pr0, pr1, pr2, pr3, tag0, ld0, dk0, tag, ld2, dk2, st0;
  v4u xr0, xr1, xr2, xr3;
#define W3(rs) "buffer_load_dwordx4 %[o0], %[" rs "], %[sw] offen lds\n\tbuffer_load_dwordx4 %[o1], %[" rs "], %[sw] offen offset:1024 lds\n\t" \
  "buffer_load_dwordx4 %[o2], %[" rs "], %[sw] offen offset:2048 lds\n\t"
  asm volatile(
      "s_load_dword %[ld], %[ldt], %[b4]\n\ts_cmp_lt_u32 %[w], 4\n\ts_cbranch_scc0 1f\n\t"
      "global_load_ushort %[p0], %[vg], %[lar] nt\n\tglobal_load_ushort %[p1], %[vg], %[aim] nt\n\t"
      "global_load_ushort %[p2], %[vg], %[cre] nt\n\tglobal_load_ushort %[p3], %[vg], %[cim] nt\n\ts_branch 5f\n\t"
      "1:\n\ts_cmp_lt_u32 %[w], 6\n\ts_cbranch_scc1 2f\n\t"
      "buffer_load_dwordx4 %[x0], %[vg], %[rx], 0 offen\n\tbuffer_load_dwordx4 %[x1], %[vg], %[rx], 0 offen offset:1024\n\t"
      "buffer_load_dwordx4 %[x2], %[vg], %[rx], 0 offen offset:2048\n\tbuffer_load_dwordx4 %[x3], %[vg], %[rx], 0 offen offset:3072\n\ts_branch 5f\n\t"
      "2:\n\ts_cmp_eq_u32 %[w], 4\n\ts_cselect_b32 m0, 0, 0x4000\n\ts_cbranch_scc1 4f\n\t"
      "3:\n\t" W3("rg") "s_add_u32 m0, m0, 0x3000\n\ts_nop 0\n\t" W3("ru")
      "s_sub_u32 m0, m0, 0x2400\n\ts_add_u32 %[sw], %[sw], 0xac00\n\ts_cmp_lt_u32 m0, 0x7000\n\ts_cbranch_scc1 3b\n\ts_branch 5f\n\t"
      "4:\n\ts_add_u32 %[t], %[sx], 0x3800\n\t"
      "buffer_load_dwordx4 %[v0], %[rx], %[sx] offen lds\n\tbuffer_load_dwordx4 %[v1], %[rx], %[sx] offen offset:1024 lds\n\t"
      "buffer_load_dwordx4 %[v0], %[rx], %[t] offen offset:2048 lds\n\tbuffer_load_dwordx4 %[v1], %[rx], %[t] offen offset:3072 lds\n\t"
      "s_add_u32 m0, m0, 0x1000\n\ts_add_u32 %[sx], %[sx], 0x8000\n\ts_cmp_lt_u32 m0, 0x4000\n\ts_cbranch_scc1 4b\n\t"
      "5:\n\ts_load_dword %[dk], %[dsk], %[b4]\n\ts_load_dword %[tg], %[ws], 0x0"
      : [p0] "=&v"(pr0), [p1] "=&v"(pr1), [p2] "=&v"(pr2), [p3] "=&v"(pr3), [x0] "=&v"(xr0), [x1] "=&v"(xr1), [x2] "=&v"(xr2), [x3] "=&v"(xr3),
        [tg] "=&s"(tag0), [ld] "=&s"(ld0), [dk] "=&s"(dk0), [t] "=&s"(st0), [sw] "+s"(sw0), [sx] "+s"(sx0)
      : [w] "s"(w), [vg] "v"(vg), [lar] "s"(ka[1]), [aim] "s"(ka[2]), [cre] "s"(ka[3]), [cim] "s"(ka[4]), [ldt] "s"(ka[5]), [dsk] "s"(ka[6]),
        [ws] "s"(p_ws), [b4] "s"(b4), [rx] "s"(rs_x), [rg] "s"(rs_g), [ru] "s"(rs_u),
        [o0] "v"(vo[0]), [o1] "v"(vo[1]), [o2] "v"(vo[2]), [v0] "v"(vx0), [v1] "v"(vx1)
      : "memory", "scc");
  fence_sched();
  auto scalars_ready = [&]() {
    asm volatile("s_waitcnt lgkmcnt(0)\n\ts_add_u32 %0, %3, 1\n\ts_mov_b32 %1, %4\n\ts_mov_b32 %2, %5"
                 : "=&s"(tag), "=&s"(ld2), "=&s"(dk2) : "s"(tag0), "s"(ld0), "s"(dk0) : "scc");
  };
  const bool freecu = ct == 15;
  const int tm = 16 * (xcd & 1) + 4 * (xcd >> 1) + (sb >> 3), tn = sb & 7;
  const int rot = (5 * tn + 11 * (tm & 3)) % 43;
  const v4u rs_ws = rsrc(p_ws, WS_END), rs_wd = rsrc(kb[1], FF * DM * 2);
  const brs wsr = __builtin_amdgcn_make_buffer_rsrc((void*)p_ws, 0, WS_END, 0x00020000);
  const u32 xres_off = (64 * tm + (tid >> 3)) * (DM * 2) + 128 * tn + 16 * (tid & 7);
  v4u xres;
  auto wd_q = [&](int i0, int i1) {
    const u32 ln = lane_id(), vq = (ln >> 3) * 1024 + 16 * ((ln & 7) ^ (4 * ((ln >> 4) & 1))) + (w & 1) * 8192, sq = tn * 128 + ((w >> 1) & 1) * (43 * 16384);
    int ph = rot + i0; ph = ph >= 43 ? ph - 43 : ph;
#pragma unroll
    for (int i = i0; i < i1; i++) { dma1(4096 * (i < 37 ? i - 10 : i - 7) + (w & 3) * 1024, vq, rs_wd, sq + ph * 16384); ph = ph == 42 ? 0 : ph + 1; }
  };

  if (w < 4) {
    __builtin_amdgcn_s_setprio(2);
    const int h = lane >> 5, l31 = lane & 31, n = lane & 15, q = lane >> 4;
    const int wm = w & 1, wn = w >> 1;
    u32 xa[2], wa[2][3];
#pragma unroll
    for (int s = 0; s < 2; s++) {
      xa[s] = (wm * 64 + n) * 128 + 16 * ((4 * (s ^ wm) + q) ^ (n >> 1));
#pragma unroll
      for (int j = 0; j < 3; j++) wa[s][j] = S_WG + (32 * (s ^ wm) + 8 * q + (n >> 2)) * 192 + 8 * (n & 3) + 32 * ((3 * wn + j) ^ (q & 1));
    }
    v4u sel[3];
#pragma unroll
    for (int p = 0; p < 3; p++) sel[p] = (v4u)((n >> 1) == 2 * p + h ? 0x3f803f80u : 0u);
    u32 sidx = (n & 1) ? 0xEEEE : 0x4444;
#define G1IN [x0] "+v"(xa[0]), [x1] "+v"(xa[1]), [w00] "+v"(wa[0][0]), [w01] "+v"(wa[0][1]), \
  [w02] "+v"(wa[0][2]), [w10] "+v"(wa[1][0]), [w11] "+v"(wa[1][1]), [w12] "+v"(wa[1][2]), [sel0] "+v"(sel[0]), [sel1] "+v"(sel[1]), [sel2] "+v"(sel[2]), [sidx] "+v"(sidx)
    {
      const u32 tb = L_KG + w * KG_W, kp = L_KPART + w * KPART, k2 = kp + (512 + 64 * q + 2 * n) * 4;
      u32 ta = tb + lane * 4, ra = tb + n * KG_PITCH + q * 16, one = 0x3f80u;
      u32 ko[4] = {kp + (16 * n + 4 * q) * 4, kp + (q < 2 ? 256 + 8 * n + 4 * q : 256 + 64 * q + 4 * n) * 4, lane <= 32 ? k2 : L_SINK + 8 * lane,
                   lane < 32 ? k2 + 128 : L_SINK + 8 * lane};
      asm volatile("" : G1IN, "+v"(ta), "+v"(ra), "+v"(one), "+v"(ko[0]), "+v"(ko[1]), "+v"(ko[2]), "+v"(ko[3]));
      scalars_ready();
      float dt = rbf(__builtin_amdgcn_exp2f((w < 2 ? bflo(ld2) : bfhi(ld2)) * LOG2E));
      asm volatile("s_waitcnt vmcnt(0)" : "+v"(pr0), "+v"(pr1), "+v"(pr2), "+v"(pr3), "+v"(dt));
      const float al = -rbf(__builtin_amdgcn_exp2f(bflo(pr0) * LOG2E)) * dt * LOG2E, be = bflo(pr1) * dt * INV2PI;
      asm volatile(
        "s_nop 0\n\t"
        "v_mov_b64 v[178:179], %[y1]\n\t"
        "v_mov_b64 v[182:183], %[v0]\n\t"
        "v_mov_b64 v[176:177], %[z1]\n\t"
        "v_mov_b64 v[180:181], %[P1]\n\t"
        "ds_write_b32 %[ta], %[one] offset:4352\n\t"
        "v_pk_mul_f32 v[184:185], v[178:179], v[178:179] op_sel_hi:[1,0]\n\t"
        "v_pk_mul_f32 v[186:187], v[182:183], v[178:179] op_sel_hi:[1,0]\n\t"
        "v_cvt_pk_bf16_f32 v164, v182, v183\n\t"
        "v_pk_fma_f32 v[184:185], v[178:179], v[178:179], v[184:185] op_sel:[1,1,0] op_sel_hi:[0,1,1] neg_lo:[1,0,0]\n\t"
        "v_pk_fma_f32 v[186:187], v[182:183], v[178:179], v[186:187] op_sel:[1,1,0] op_sel_hi:[0,1,1] neg_lo:[1,0,0]\n\t"
        "ds_write_b32 %[ta], v164 offset:0\n\t"
        "v_pk_mul_f32 v[188:189], v[184:185], v[184:185] op_sel_hi:[1,0]\n\t"
        "v_pk_mul_f32 v[190:191], v[182:183], v[184:185] op_sel_hi:[1,0]\n\t"
        "v_pk_mul_f32 v[192:193], v[186:187], v[184:185] op_sel_hi:[1,0]\n\t"
        "v_pk_fma_f32 v[188:189], v[184:185], v[184:185], v[188:189] op_sel:[1,1,0] op_sel_hi:[0,1,1] neg_lo:[1,0,0]\n\t"
        "v_pk_fma_f32 v[190:191], v[182:183], v[184:185], v[190:191] op_sel:[1,1,0] op_sel_hi:[0,1,1] neg_lo:[1,0,0]\n\t"
        "v_pk_fma_f32 v[192:193], v[186:187], v[184:185], v[192:193] op_sel:[1,1,0] op_sel_hi:[0,1,1] neg_lo:[1,0,0]\n\t"
        "v_cvt_pk_bf16_f32 v165, v186, v187\n\t"
        "v_cvt_pk_bf16_f32 v166, v190, v191\n\t"
        "v_cvt_pk_bf16_f32 v167, v192, v193\n\t"
        "ds_write_b32 %[ta], v165 offset:272\n\t"
        "v_pk_mul_f32 v[194:195], v[182:183], v[188:189] op_sel_hi:[1,0]\n\t"
        "ds_write_b32 %[ta], v166 offset:544\n\t"
        "v_pk_mul_f32 v[196:197], v[186:187], v[188:189] op_sel_hi:[1,0]\n\t"
        "ds_write_b32 %[ta], v167 offset:816\n\t"
        "v_pk_fma_f32 v[194:195], v[182:183], v[188:189], v[194:195] op_sel:[1,1,0] op_sel_hi:[0,1,1] neg_lo:[1,0,0]\n\t"
        "v_pk_fma_f32 v[196:197], v[186:187], v[188:189], v[196:197] op_sel:[1,1,0] op_sel_hi:[0,1,1] neg_lo:[1,0,0]\n\t"
        "v_pk_mul_f32 v[198:199], v[190:191], v[188:189] op_sel_hi:[1,0]\n\t"
        "v_cvt_pk_bf16_f32 v168, v194, v195\n\t"
        "v_cvt_pk_bf16_f32 v169, v196, v197\n\t"
        "v_pk_fma_f32 v[198:199], v[190:191], v[188:189], v[198:199] op_sel:[1,1,0] op_sel_hi:[0,1,1] neg_lo:[1,0,0]\n\t"
        "ds_write_b32 %[ta], v168 offset:1088\n\t"
        "v_pk_mul_f32 v[200:201], v[192:193], v[188:189] op_sel_hi:[1,0]\n\t"
        "ds_write_b32 %[ta], v169 offset:1360\n\t"
        "v_cvt_pk_bf16_f32 v170, v198, v199\n\t"
        "v_pk_fma_f32 v[200:201], v[192:193], v[188:189], v[200:201] op_sel:[1,1,0] op_sel_hi:[0,1,1] neg_lo:[1,0,0]\n\t"
        "v_pk_mul_f32 v[182:183], v[194:195], v[188:189] op_sel_hi:[1,0]\n\t"
        "ds_write_b32 %[ta], v170 offset:1632\n\t"
        "v_cvt_pk_bf16_f32 v171, v200, v201\n\t"
        "v_pk_fma_f32 v[182:183], v[194:195], v[188:189], v[182:183] op_sel:[1,1,0] op_sel_hi:[0,1,1] neg_lo:[1,0,0]\n\t"
        "v_pk_mul_f32 v[186:187], v[196:197], v[188:189] op_sel_hi:[1,0]\n\t"
        "ds_write_b32 %[ta], v171 offset:1904\n\t"
        "v_cvt_pk_bf16_f32 v172, v182, v183\n\t"
        "v_pk_fma_f32 v[186:187], v[196:197], v[188:189], v[186:187] op_sel:[1,1,0] op_sel_hi:[0,1,1] neg_lo:[1,0,0]\n\t"
        "v_pk_mul_f32 v[190:191], v[198:199], v[188:189] op_sel_hi:[1,0]\n\t"
        "ds_write_b32 %[ta], v172 offset:2176\n\t"
        "v_cvt_pk_bf16_f32 v173, v186, v187\n\t"
        "v_pk_fma_f32 v[190:191], v[198:199], v[188:189], v[190:191] op_sel:[1,1,0] op_sel_hi:[0,1,1] neg_lo:[1,0,0]\n\t"
        "v_pk_mul_f32 v[192:193], v[200:201], v[188:189] op_sel_hi:[1,0]\n\t"
        "ds_write_b32 %[ta], v173 offset:2448\n\t"
        "v_cvt_pk_bf16_f32 v174, v190, v191\n\t"
        "v_pk_fma_f32 v[192:193], v[200:201], v[188:189], v[192:193] op_sel:[1,1,0] op_sel_hi:[0,1,1] neg_lo:[1,0,0]\n\t"
        "v_pk_mul_f32 v[194:195], v[182:183], v[188:189] op_sel_hi:[1,0]\n\t"
        "ds_write_b32 %[ta], v174 offset:2720\n\t"
        "v_cvt_pk_bf16_f32 v175, v192, v193\n\t"
        "v_pk_fma_f32 v[194:195], v[182:183], v[188:189], v[194:195] op_sel:[1,1,0] op_sel_hi:[0,1,1] neg_lo:[1,0,0]\n\t"
        "v_pk_mul_f32 v[196:197], v[186:187], v[188:189] op_sel_hi:[1,0]\n\t"
        "ds_write_b32 %[ta], v175 offset:2992\n\t"
        "v_cvt_pk_bf16_f32 v164, v194, v195\n\t"
        "v_pk_fma_f32 v[196:197], v[186:187], v[188:189], v[196:197] op_sel:[1,1,0] op_sel_hi:[0,1,1] neg_lo:[1,0,0]\n\t"
        "v_pk_mul_f32 v[198:199], v[190:191], v[188:189] op_sel_hi:[1,0]\n\t"
        "ds_write_b32 %[ta], v164 offset:3264\n\t"
        "v_cvt_pk_bf16_f32 v165, v196, v197\n\t"
        "v_pk_fma_f32 v[198:199], v[190:191], v[188:189], v[198:199] op_sel:[1,1,0] op_sel_hi:[0,1,1] neg_lo:[1,0,0]\n\t"
        "v_pk_mul_f32 v[200:201], v[192:193], v[188:189] op_sel_hi:[1,0]\n\t"
        "ds_write_b32 %[ta], v165 offset:3536\n\t"
        "v_cvt_pk_bf16_f32 v166, v198, v199\n\t"
        "v_pk_fma_f32 v[200:201], v[192:193], v[188:189], v[200:201] op_sel:[1,1,0] op_sel_hi:[0,1,1] neg_lo:[1,0,0]\n\t"
        "v_cvt_pk_bf16_f32 v167, v176, v177\n\t"
        "ds_write_b32 %[ta], v166 offset:3808\n\t"
        "v_cvt_pk_bf16_f32 v168, v200, v201\n\t"
        "ds_write_b32 %[ta], v167 offset:4624\n\t"
        "v_pk_mul_f32 v[202:203], v[176:177], v[176:177] op_sel_hi:[1,0]\n\t"
        "ds_write_b32 %[ta], v168 offset:4080\n\t"
        "v_pk_mul_f32 v[230:231], v[180:181], v[180:181] op_sel_hi:[1,0]\n\t"
        "v_pk_fma_f32 v[202:203], v[176:177], v[176:177], v[202:203] op_sel:[1,1,0] op_sel_hi:[0,1,1] neg_lo:[1,0,0]\n\t"
        "ds_read_b128 v[100:103], %[ra] offset:0\n\t"
        "v_pk_fma_f32 v[230:231], v[180:181], v[180:181], v[230:231] op_sel:[1,1,0] op_sel_hi:[0,1,1] neg_lo:[1,0,0]\n\t"
        "ds_read_b128 v[104:107], %[ra] offset:64\n\t"
        "v_cvt_pk_bf16_f32 v169, v202, v203\n\t"
        "ds_read_b128 v[108:111], %[ra] offset:128\n\t"
        "v_pk_mul_f32 v[204:205], v[176:177], v[202:203] op_sel_hi:[1,0]\n\t"
        "ds_write_b32 %[ta], v169 offset:4896\n\t"
        "v_pk_mul_f32 v[206:207], v[202:203], v[202:203] op_sel_hi:[1,0]\n\t"
        "ds_read_b128 v[112:115], %[ra] offset:192\n\t"
        "v_pk_fma_f32 v[204:205], v[176:177], v[202:203], v[204:205] op_sel:[1,1,0] op_sel_hi:[0,1,1] neg_lo:[1,0,0]\n\t"
        "v_pk_fma_f32 v[206:207], v[202:203], v[202:203], v[206:207] op_sel:[1,1,0] op_sel_hi:[0,1,1] neg_lo:[1,0,0]\n\t"
        "v_pk_mul_f32 v[232:233], v[180:181], v[230:231] op_sel_hi:[1,0]\n\t"
        "v_cvt_pk_bf16_f32 v170, v204, v205\n\t"
        "v_cvt_pk_bf16_f32 v171, v206, v207\n\t"
        "v_pk_mul_f32 v[208:209], v[202:203], v[204:205] op_sel_hi:[1,0]\n\t"
        "ds_write_b32 %[ta], v170 offset:5168\n\t"
        "v_pk_mul_f32 v[210:211], v[204:205], v[204:205] op_sel_hi:[1,0]\n\t"
        "ds_write_b32 %[ta], v171 offset:5440\n\t"
        "v_pk_fma_f32 v[208:209], v[202:203], v[204:205], v[208:209] op_sel:[1,1,0] op_sel_hi:[0,1,1] neg_lo:[1,0,0]\n\t"
        "v_pk_fma_f32 v[210:211], v[204:205], v[204:205], v[210:211] op_sel:[1,1,0] op_sel_hi:[0,1,1] neg_lo:[1,0,0]\n\t"
        "v_pk_mul_f32 v[212:213], v[204:205], v[206:207] op_sel_hi:[1,0]\n\t"
        "v_cvt_pk_bf16_f32 v172, v208, v209\n\t"
        "v_cvt_pk_bf16_f32 v173, v210, v211\n\t"
        "v_pk_fma_f32 v[212:213], v[204:205], v[206:207], v[212:213] op_sel:[1,1,0] op_sel_hi:[0,1,1] neg_lo:[1,0,0]\n\t"
        "ds_write_b32 %[ta], v172 offset:5712\n\t"
        "v_pk_mul_f32 v[214:215], v[206:207], v[206:207] op_sel_hi:[1,0]\n\t"
        "ds_write_b32 %[ta], v173 offset:5984\n\t"
        "v_cvt_pk_bf16_f32 v174, v212, v213\n\t"
        "v_pk_fma_f32 v[214:215], v[206:207], v[206:207], v[214:215] op_sel:[1,1,0] op_sel_hi:[0,1,1] neg_lo:[1,0,0]\n\t"
        "v_pk_mul_f32 v[216:217], v[206:207], v[208:209] op_sel_hi:[1,0]\n\t"
        "ds_write_b32 %[ta], v174 offset:6256\n\t"
        "v_cvt_pk_bf16_f32 v175, v214, v215\n\t"
        "v_pk_fma_f32 v[216:217], v[206:207], v[208:209], v[216:217] op_sel:[1,1,0] op_sel_hi:[0,1,1] neg_lo:[1,0,0]\n\t"
        "v_pk_mul_f32 v[218:219], v[208:209], v[208:209] op_sel_hi:[1,0]\n\t"
        "ds_write_b32 %[ta], v175 offset:6528\n\t"
        "v_cvt_pk_bf16_f32 v164, v216, v217\n\t"
        "v_pk_fma_f32 v[218:219], v[208:209], v[208:209], v[218:219] op_sel:[1,1,0] op_sel_hi:[0,1,1] neg_lo:[1,0,0]\n\t"
        "v_pk_mul_f32 v[220:221], v[208:209], v[210:211] op_sel_hi:[1,0]\n\t"
        "ds_write_b32 %[ta], v164 offset:6800\n\t"
        "v_cvt_pk_bf16_f32 v165, v218, v219\n\t"
        "v_pk_fma_f32 v[220:221], v[208:209], v[210:211], v[220:221] op_sel:[1,1,0] op_sel_hi:[0,1,1] neg_lo:[1,0,0]\n\t"
        "v_pk_mul_f32 v[222:223], v[210:211], v[210:211] op_sel_hi:[1,0]\n\t"
        "ds_write_b32 %[ta], v165 offset:7072\n\t"
        "v_cvt_pk_bf16_f32 v166, v220, v221\n\t"
        "v_pk_fma_f32 v[222:223], v[210:211], v[210:211], v[222:223] op_sel:[1,1,0] op_sel_hi:[0,1,1] neg_lo:[1,0,0]\n\t"
        "v_pk_mul_f32 v[224:225], v[210:211], v[212:213] op_sel_hi:[1,0]\n\t"
        "ds_write_b32 %[ta], v166 offset:7344\n\t"
        "v_cvt_pk_bf16_f32 v167, v222, v223\n\t"
        "v_pk_fma_f32 v[224:225], v[210:211], v[212:213], v[224:225] op_sel:[1,1,0] op_sel_hi:[0,1,1] neg_lo:[1,0,0]\n\t"
        "v_pk_mul_f32 v[226:227], v[212:213], v[212:213] op_sel_hi:[1,0]\n\t"
        "ds_write_b32 %[ta], v167 offset:7616\n\t"
        "v_cvt_pk_bf16_f32 v168, v224, v225\n\t"
        "v_pk_fma_f32 v[226:227], v[212:213], v[212:213], v[226:227] op_sel:[1,1,0] op_sel_hi:[0,1,1] neg_lo:[1,0,0]\n\t"
        "v_pk_mul_f32 v[228:229], v[212:213], v[214:215] op_sel_hi:[1,0]\n\t"
        "ds_write_b32 %[ta], v168 offset:7888\n\t"
        "v_cvt_pk_bf16_f32 v169, v226, v227\n\t"
        "v_pk_fma_f32 v[228:229], v[212:213], v[214:215], v[228:229] op_sel:[1,1,0] op_sel_hi:[0,1,1] neg_lo:[1,0,0]\n\t"
        "v_pk_fma_f32 v[232:233], v[180:181], v[230:231], v[232:233] op_sel:[1,1,0] op_sel_hi:[0,1,1] neg_lo:[1,0,0]\n\t"
        "ds_write_b32 %[ta], v169 offset:8160\n\t"
        "v_cvt_pk_bf16_f32 v170, v228, v229\n\t"
        "v_pk_mul_f32 v[234:235], v[230:231], v[230:231] op_sel_hi:[1,0]\n\t"
        "v_pk_mul_f32 v[236:237], v[230:231], v[232:233] op_sel_hi:[1,0]\n\t"
        "ds_write_b32 %[ta], v170 offset:8432\n\t"
        "v_pk_fma_f32 v[234:235], v[230:231], v[230:231], v[234:235] op_sel:[1,1,0] op_sel_hi:[0,1,1] neg_lo:[1,0,0]\n\t"
        "v_pk_fma_f32 v[236:237], v[230:231], v[232:233], v[236:237] op_sel:[1,1,0] op_sel_hi:[0,1,1] neg_lo:[1,0,0]\n\t"
        "ds_read_b128 v[116:119], %[ra] offset:4352\n\t"
        "v_pk_mul_f32 v[238:239], v[232:233], v[232:233] op_sel_hi:[1,0]\n\t"
        "ds_read_b128 v[120:123], %[ra] offset:4416\n\t"
        "v_pk_mul_f32 v[240:241], v[232:233], v[234:235] op_sel_hi:[1,0]\n\t"
        "ds_read_b128 v[124:127], %[ra] offset:4480\n\t"
        "v_pk_fma_f32 v[238:239], v[232:233], v[232:233], v[238:239] op_sel:[1,1,0] op_sel_hi:[0,1,1] neg_lo:[1,0,0]\n\t"
        "ds_read_b128 v[128:131], %[ra] offset:4544\n\t"
        "v_pk_fma_f32 v[240:241], v[232:233], v[234:235], v[240:241] op_sel:[1,1,0] op_sel_hi:[0,1,1] neg_lo:[1,0,0]\n\t"
        "v_pk_mul_f32 v[242:243], v[234:235], v[234:235] op_sel_hi:[1,0]\n\t"
        "v_cvt_pk_bf16_f32 v171, v180, v181\n\t"
        "v_pk_mul_f32 v[244:245], v[180:181], v[202:203] op_sel_hi:[1,0]\n\t"
        "v_pk_fma_f32 v[242:243], v[234:235], v[234:235], v[242:243] op_sel:[1,1,0] op_sel_hi:[0,1,1] neg_lo:[1,0,0]\n\t"
        "ds_write_b32 %[ta], v171 offset:8704\n\t"
        "v_pk_fma_f32 v[244:245], v[180:181], v[202:203], v[244:245] op_sel:[1,1,0] op_sel_hi:[0,1,1] neg_lo:[1,0,0]\n\t"
        "v_pk_mul_f32 v[246:247], v[180:181], v[206:207] op_sel_hi:[1,0]\n\t"
        "v_pk_mul_f32 v[248:249], v[180:181], v[210:211] op_sel_hi:[1,0]\n\t"
        "v_cvt_pk_bf16_f32 v172, v244, v245\n\t"
        "v_pk_fma_f32 v[246:247], v[180:181], v[206:207], v[246:247] op_sel:[1,1,0] op_sel_hi:[0,1,1] neg_lo:[1,0,0]\n\t"
        "v_pk_fma_f32 v[248:249], v[180:181], v[210:211], v[248:249] op_sel:[1,1,0] op_sel_hi:[0,1,1] neg_lo:[1,0,0]\n\t"
        "ds_write_b32 %[ta], v172 offset:8976\n\t"
        "v_cvt_pk_bf16_f32 v173, v246, v247\n\t"
        "v_cvt_pk_bf16_f32 v174, v248, v249\n\t"
        "v_pk_mul_f32 v[250:251], v[180:181], v[214:215] op_sel_hi:[1,0]\n\t"
        "ds_write_b32 %[ta], v173 offset:9248\n\t"
        "v_pk_mul_f32 v[244:245], v[180:181], v[218:219] op_sel_hi:[1,0]\n\t"
        "ds_write_b32 %[ta], v174 offset:9520\n\t"
        "v_pk_fma_f32 v[250:251], v[180:181], v[214:215], v[250:251] op_sel:[1,1,0] op_sel_hi:[0,1,1] neg_lo:[1,0,0]\n\t"
        "v_pk_fma_f32 v[244:245], v[180:181], v[218:219], v[244:245] op_sel:[1,1,0] op_sel_hi:[0,1,1] neg_lo:[1,0,0]\n\t"
        "v_pk_mul_f32 v[246:247], v[180:181], v[222:223] op_sel_hi:[1,0]\n\t"
        "v_cvt_pk_bf16_f32 v175, v250, v251\n\t"
        "v_cvt_pk_bf16_f32 v164, v244, v245\n\t"
        "v_pk_fma_f32 v[246:247], v[180:181], v[222:223], v[246:247] op_sel:[1,1,0] op_sel_hi:[0,1,1] neg_lo:[1,0,0]\n\t"
        "ds_write_b32 %[ta], v175 offset:9792\n\t"
        "v_pk_mul_f32 v[248:249], v[180:181], v[226:227] op_sel_hi:[1,0]\n\t"
        "ds_write_b32 %[ta], v164 offset:10064\n\t"
        "v_cvt_pk_bf16_f32 v165, v246, v247\n\t"
        "v_pk_fma_f32 v[248:249], v[180:181], v[226:227], v[248:249] op_sel:[1,1,0] op_sel_hi:[0,1,1] neg_lo:[1,0,0]\n\t"
        "v_cvt_pk_bf16_f32 v166, v230, v231\n\t"
        "ds_write_b32 %[ta], v165 offset:10336\n\t"
        "v_cvt_pk_bf16_f32 v167, v248, v249\n\t"
        "ds_write_b32 %[ta], v166 offset:10880\n\t"
        "v_pk_mul_f32 v[250:251], v[230:231], v[206:207] op_sel_hi:[1,0]\n\t"
        "ds_write_b32 %[ta], v167 offset:10608\n\t"
        "v_pk_mul_f32 v[244:245], v[230:231], v[214:215] op_sel_hi:[1,0]\n\t"
        "v_pk_fma_f32 v[250:251], v[230:231], v[206:207], v[250:251] op_sel:[1,1,0] op_sel_hi:[0,1,1] neg_lo:[1,0,0]\n\t"
        "v_pk_mul_f32 v[246:247], v[230:231], v[222:223] op_sel_hi:[1,0]\n\t"
        "v_pk_fma_f32 v[244:245], v[230:231], v[214:215], v[244:245] op_sel:[1,1,0] op_sel_hi:[0,1,1] neg_lo:[1,0,0]\n\t"
        "v_cvt_pk_bf16_f32 v168, v250, v251\n\t"
        "v_pk_fma_f32 v[246:247], v[230:231], v[222:223], v[246:247] op_sel:[1,1,0] op_sel_hi:[0,1,1] neg_lo:[1,0,0]\n\t"
        "v_cvt_pk_bf16_f32 v169, v244, v245\n\t"
        "ds_write_b32 %[ta], v168 offset:11152\n\t"
        "v_cvt_pk_bf16_f32 v170, v246, v247\n\t"
        "ds_write_b32 %[ta], v169 offset:11424\n\t"
        "v_cvt_pk_bf16_f32 v171, v232, v233\n\t"
        "ds_write_b32 %[ta], v170 offset:11696\n\t"
        "v_pk_mul_f32 v[248:249], v[232:233], v[206:207] op_sel_hi:[1,0]\n\t"
        "ds_write_b32 %[ta], v171 offset:11968\n\t"
        "v_pk_mul_f32 v[250:251], v[232:233], v[214:215] op_sel_hi:[1,0]\n\t"
        "v_pk_fma_f32 v[248:249], v[232:233], v[206:207], v[248:249] op_sel:[1,1,0] op_sel_hi:[0,1,1] neg_lo:[1,0,0]\n\t"
        "v_pk_mul_f32 v[244:245], v[232:233], v[222:223] op_sel_hi:[1,0]\n\t"
        "v_pk_fma_f32 v[250:251], v[232:233], v[214:215], v[250:251] op_sel:[1,1,0] op_sel_hi:[0,1,1] neg_lo:[1,0,0]\n\t"
        "v_cvt_pk_bf16_f32 v172, v248, v249\n\t"
        "v_pk_fma_f32 v[244:245], v[232:233], v[222:223], v[244:245] op_sel:[1,1,0] op_sel_hi:[0,1,1] neg_lo:[1,0,0]\n\t"
        "v_cvt_pk_bf16_f32 v173, v250, v251\n\t"
        "ds_write_b32 %[ta], v172 offset:12240\n\t"
        "v_cvt_pk_bf16_f32 v174, v244, v245\n\t"
        "ds_write_b32 %[ta], v173 offset:12512\n\t"
        "v_cvt_pk_bf16_f32 v175, v234, v235\n\t"
        "ds_write_b32 %[ta], v174 offset:12784\n\t"
        "v_pk_mul_f32 v[246:247], v[234:235], v[214:215] op_sel_hi:[1,0]\n\t"
        "ds_write_b32 %[ta], v175 offset:13056\n\t"
        "v_cvt_pk_bf16_f32 v164, v236, v237\n\t"
        "ds_read_b128 v[132:135], %[ra] offset:8704\n\t"
        "v_pk_fma_f32 v[246:247], v[234:235], v[214:215], v[246:247] op_sel:[1,1,0] op_sel_hi:[0,1,1] neg_lo:[1,0,0]\n\t"
        "ds_write_b32 %[ta], v164 offset:13600\n\t"
        "v_pk_mul_f32 v[248:249], v[236:237], v[214:215] op_sel_hi:[1,0]\n\t"
        "ds_read_b128 v[136:139], %[ra] offset:8768\n\t"
        "v_cvt_pk_bf16_f32 v165, v246, v247\n\t"
        "ds_read_b128 v[140:143], %[ra] offset:8832\n\t"
        "v_pk_fma_f32 v[248:249], v[236:237], v[214:215], v[248:249] op_sel:[1,1,0] op_sel_hi:[0,1,1] neg_lo:[1,0,0]\n\t"
        "ds_write_b32 %[ta], v165 offset:13328\n\t"
        "v_cvt_pk_bf16_f32 v166, v238, v239\n\t"
        "ds_read_b128 v[144:147], %[ra] offset:8896\n\t"
        "v_cvt_pk_bf16_f32 v167, v248, v249\n\t"
        "ds_write_b32 %[ta], v166 offset:14144\n\t"
        "v_pk_mul_f32 v[250:251], v[238:239], v[214:215] op_sel_hi:[1,0]\n\t"
        "ds_write_b32 %[ta], v167 offset:13872\n\t"
        "v_cvt_pk_bf16_f32 v168, v240, v241\n\t"
        "v_pk_fma_f32 v[250:251], v[238:239], v[214:215], v[250:251] op_sel:[1,1,0] op_sel_hi:[0,1,1] neg_lo:[1,0,0]\n\t"
        "v_pk_mul_f32 v[244:245], v[240:241], v[214:215] op_sel_hi:[1,0]\n\t"
        "ds_write_b32 %[ta], v168 offset:14688\n\t"
        "v_cvt_pk_bf16_f32 v169, v250, v251\n\t"
        "v_pk_fma_f32 v[244:245], v[240:241], v[214:215], v[244:245] op_sel:[1,1,0] op_sel_hi:[0,1,1] neg_lo:[1,0,0]\n\t"
        "v_cvt_pk_bf16_f32 v170, v242, v243\n\t"
        "ds_write_b32 %[ta], v169 offset:14416\n\t"
        "v_cvt_pk_bf16_f32 v171, v244, v245\n\t"
        "ds_write_b32 %[ta], v170 offset:15232\n\t"
        "ds_write_b32 %[ta], v171 offset:14960\n\t"
        "ds_read_b128 v[148:151], %[ra] offset:13056\n\t"
        "ds_read_b128 v[152:155], %[ra] offset:13120\n\t"
        "ds_read_b128 v[156:159], %[ra] offset:13184\n\t"
        "ds_read_b128 v[160:163], %[ra] offset:13248\n\t"
        "s_waitcnt lgkmcnt(4)\n\t"
        "v_mfma_f32_16x16x32_bf16 v[164:167], v[116:119], v[100:103], 0\n\t"
        "v_mfma_f32_16x16x32_bf16 v[164:167], v[120:123], v[104:107], v[164:167]\n\t"
        "v_mfma_f32_16x16x32_bf16 v[164:167], v[124:127], v[108:111], v[164:167]\n\t"
        "v_mfma_f32_16x16x32_bf16 v[164:167], v[128:131], v[112:115], v[164:167]\n\t"
        "s_waitcnt lgkmcnt(4)\n\t"
        "v_mfma_f32_16x16x32_bf16 v[168:171], v[132:135], v[100:103], 0\n\t"
        "v_mfma_f32_16x16x32_bf16 v[168:171], v[136:139], v[104:107], v[168:171]\n\t"
        "v_mfma_f32_16x16x32_bf16 v[168:171], v[140:143], v[108:111], v[168:171]\n\t"
        "v_mfma_f32_16x16x32_bf16 v[168:171], v[144:147], v[112:115], v[168:171]\n\t"
        "s_waitcnt lgkmcnt(0)\n\t"
        "v_mfma_f32_16x16x32_bf16 v[172:175], v[148:151], v[100:103], 0\n\t"
        "v_mfma_f32_16x16x32_bf16 v[172:175], v[152:155], v[104:107], v[172:175]\n\t"
        "v_mfma_f32_16x16x32_bf16 v[172:175], v[156:159], v[108:111], v[172:175]\n\t"
        "v_mfma_f32_16x16x32_bf16 v[172:175], v[160:163], v[112:115], v[172:175]\n\t"
        "ds_write_b128 %[k0], v[164:167]\n\t"
        "s_nop 2\n\t"
        "ds_write_b128 %[k1], v[168:171]\n\t"
        "s_nop 2\n\t"
        "ds_write_b64 %[k2], v[172:173]\n\t"
        "ds_write_b64 %[k3], v[174:175]\n\t"
        :
        : [z1] "v"(cexp(al, be, 1.f)), [y1] "v"(cexp(al, -be, 16.f)), [P1] "v"(cexp(al, be, 256.f)),
          [v0] "v"((f32x2){2.f * dt * bflo(pr2), -2.f * dt * bflo(pr3)}), [ta] "v"(ta), [ra] "v"(ra), [one] "v"(one),
          [k0] "v"(ko[0]), [k1] "v"(ko[1]), [k2] "v"(ko[2]), [k3] "v"(ko[3])
        : "memory",
          "v100", "v101", "v102", "v103", "v104", "v105", "v106", "v107", "v108", "v109", "v110", "v111", "v112", "v113", "v114", "v115",
          "v116", "v117", "v118", "v119", "v120", "v121", "v122", "v123", "v124", "v125", "v126", "v127", "v128", "v129", "v130", "v131",
          "v132", "v133", "v134", "v135", "v136", "v137", "v138", "v139", "v140", "v141", "v142", "v143", "v144", "v145", "v146", "v147",
          "v148", "v149", "v150", "v151", "v152", "v153", "v154", "v155", "v156", "v157", "v158", "v159", "v160", "v161", "v162", "v163",
          "v164", "v165", "v166", "v167", "v168", "v169", "v170", "v171", "v172", "v173", "v174", "v175", "v176", "v177", "v178", "v179",
          "v180", "v181", "v182", "v183", "v184", "v185", "v186", "v187", "v188", "v189", "v190", "v191", "v192", "v193", "v194", "v195",
          "v196", "v197", "v198", "v199", "v200", "v201", "v202", "v203", "v204", "v205", "v206", "v207", "v208", "v209", "v210", "v211",
          "v212", "v213", "v214", "v215", "v216", "v217", "v218", "v219", "v220", "v221", "v222", "v223", "v224", "v225", "v226", "v227",
          "v228", "v229", "v230", "v231", "v232", "v233", "v234", "v235", "v236", "v237", "v238", "v239", "v240", "v241", "v242", "v243",
          "v244", "v245", "v246", "v247", "v248", "v249", "v250", "v251");
    }
    bar();
    if (!freecu) {
      u32 cnt = 0, dlt;
      f32x4 a[6][4];
      f32x2 cl[6], ch[6];
      float mn[4], sd[4], sc[4];
#define SO(t) [mn##t] "=&v"(mn[t]), [sd##t] "=&v"(sd[t]), [sc##t] "=&v"(sc[t])
#define AO(j) [a##j##0] "=&v"(a[j][0]), [a##j##1] "=&v"(a[j][1]), [a##j##2] "=&v"(a[j][2]), [a##j##3] "=&v"(a[j][3]), [cl##j] "=v"(cl[j]), [ch##j] "=v"(ch[j])
      asm volatile(
        "ds_read_b128 v[224:227], %[x0] offset:0\n\t"
        "ds_read_b128 v[228:231], %[x0] offset:2048\n\t"
        "ds_read_b64_tr_b16 v[200:201], %[w00] offset:0\n\t"
        "ds_read_b64_tr_b16 v[202:203], %[w00] offset:768\n\t"
        "ds_read_b128 v[232:235], %[x0] offset:4096\n\t"
        "ds_read_b128 v[236:239], %[x0] offset:6144\n\t"
        "ds_read_b64_tr_b16 v[204:205], %[w00] offset:12288\n\t"
        "ds_read_b64_tr_b16 v[206:207], %[w00] offset:13056\n\t"
        "ds_read_b64_tr_b16 v[208:209], %[w01] offset:0\n\t"
        "ds_read_b64_tr_b16 v[210:211], %[w01] offset:768\n\t"
        "ds_read_b64_tr_b16 v[212:213], %[w01] offset:12288\n\t"
        "ds_read_b64_tr_b16 v[214:215], %[w01] offset:13056\n\t"
        "ds_read_b64_tr_b16 v[216:217], %[w02] offset:0\n\t"
        "ds_read_b64_tr_b16 v[218:219], %[w02] offset:768\n\t"
        "v_mov_b64 v[196:197], 0\n\t"
        "v_mov_b32 v198, %[one]\n\t"
        "v_mov_b32 v199, 0\n\t"
        "s_mov_b32 %[d], %[dp]\n\t"
        "s_waitcnt lgkmcnt(6)\n\t"
        "v_mfma_f32_16x16x32_bf16 %[a00], v[200:203], v[224:227], 0\n\t"
        "ds_read_b64_tr_b16 v[220:221], %[w02] offset:12288\n\t"
        "v_mfma_f32_16x16x32_bf16 %[a01], v[200:203], v[228:231], 0\n\t"
        "ds_read_b64_tr_b16 v[222:223], %[w02] offset:13056\n\t"
        "v_mfma_f32_16x16x32_bf16 %[a02], v[200:203], v[232:235], 0\n\t"
        "v_add_u32 %[x0], %[d], %[x0]\n\t"
        "v_mfma_f32_16x16x32_bf16 %[a03], v[200:203], v[236:239], 0\n\t"
        "v_add_u32 %[w00], %[d], %[w00]\n\t"
        "v_mfma_f32_16x16x32_bf16 %[a13], v[204:207], v[236:239], 0\n\t"
        "v_add_u32 %[w01], %[d], %[w01]\n\t"
        "v_mfma_f32_16x16x32_bf16 %[a12], v[204:207], v[232:235], 0\n\t"
        "v_add_u32 %[w02], %[d], %[w02]\n\t"
        "v_mfma_f32_16x16x32_bf16 %[a11], v[204:207], v[228:231], 0\n\t"
        "v_smfmac_f32_16x16x64_bf16 v[196:199], %[sel0], v[200:207], %[sidx]\n\t"
        "v_mfma_f32_16x16x32_bf16 %[a10], v[204:207], v[224:227], 0\n\t"
        "s_waitcnt lgkmcnt(0)\n\t"
        "v_mfma_f32_16x16x32_bf16 %[a20], v[208:211], v[224:227], 0\n\t"
        "ds_read_b128 v[240:243], %[x1] offset:0\n\t"
        "v_mfma_f32_16x16x32_bf16 %[a21], v[208:211], v[228:231], 0\n\t"
        "ds_read_b128 v[244:247], %[x1] offset:2048\n\t"
        "v_mfma_f32_16x16x32_bf16 %[a22], v[208:211], v[232:235], 0\n\t"
        "ds_read_b64_tr_b16 v[200:201], %[w10] offset:0\n\t"
        "v_mfma_f32_16x16x32_bf16 %[a23], v[208:211], v[236:239], 0\n\t"
        "ds_read_b64_tr_b16 v[202:203], %[w10] offset:768\n\t"
        "v_mfma_f32_16x16x32_bf16 %[a33], v[212:215], v[236:239], 0\n\t"
        "ds_read_b128 v[248:251], %[x1] offset:4096\n\t"
        "v_mfma_f32_16x16x32_bf16 %[a32], v[212:215], v[232:235], 0\n\t"
        "ds_read_b128 v[252:255], %[x1] offset:6144\n\t"
        "v_mfma_f32_16x16x32_bf16 %[a31], v[212:215], v[228:231], 0\n\t"
        "ds_read_b64_tr_b16 v[204:205], %[w10] offset:12288\n\t"
        "v_mfma_f32_16x16x32_bf16 %[a30], v[212:215], v[224:227], 0\n\t"
        "ds_read_b64_tr_b16 v[206:207], %[w10] offset:13056\n\t"
        "v_smfmac_f32_16x16x64_bf16 v[196:199], %[sel1], v[208:215], %[sidx]\n\t"
        "v_mfma_f32_16x16x32_bf16 %[a40], v[216:219], v[224:227], 0\n\t"
        "ds_read_b64_tr_b16 v[208:209], %[w11] offset:0\n\t"
        "v_mfma_f32_16x16x32_bf16 %[a41], v[216:219], v[228:231], 0\n\t"
        "ds_read_b64_tr_b16 v[210:211], %[w11] offset:768\n\t"
        "v_mfma_f32_16x16x32_bf16 %[a42], v[216:219], v[232:235], 0\n\t"
        "ds_read_b64_tr_b16 v[212:213], %[w11] offset:12288\n\t"
        "v_mfma_f32_16x16x32_bf16 %[a43], v[216:219], v[236:239], 0\n\t"
        "ds_read_b64_tr_b16 v[214:215], %[w11] offset:13056\n\t"
        "v_smfmac_f32_16x16x64_bf16 v[196:199], %[sel2], v[216:223], %[sidx]\n\t"
        "v_mfma_f32_16x16x32_bf16 %[a53], v[220:223], v[236:239], 0\n\t"
        "ds_read_b64_tr_b16 v[216:217], %[w12] offset:0\n\t"
        "v_mfma_f32_16x16x32_bf16 %[a52], v[220:223], v[232:235], 0\n\t"
        "ds_read_b64_tr_b16 v[218:219], %[w12] offset:768\n\t"
        "v_mfma_f32_16x16x32_bf16 %[a51], v[220:223], v[228:231], 0\n\t"
        "v_add_u32 %[x1], %[d], %[x1]\n\t"
        "v_mfma_f32_16x16x32_bf16 %[a50], v[220:223], v[224:227], 0\n\t"
        "v_add_u32 %[w10], %[d], %[w10]\n\t"
        "1:\n\t"
        "s_waitcnt lgkmcnt(6)\n\t"
        "v_mfma_f32_16x16x32_bf16 %[a00], v[200:203], v[240:243], %[a00]\n\t"
        "ds_read_b64_tr_b16 v[220:221], %[w12] offset:12288\n\t"
        "v_mfma_f32_16x16x32_bf16 %[a01], v[200:203], v[244:247], %[a01]\n\t"
        "ds_read_b64_tr_b16 v[222:223], %[w12] offset:13056\n\t"
        "v_mfma_f32_16x16x32_bf16 %[a02], v[200:203], v[248:251], %[a02]\n\t"
        "v_add_u32 %[w11], %[d], %[w11]\n\t"
        "v_mfma_f32_16x16x32_bf16 %[a03], v[200:203], v[252:255], %[a03]\n\t"
        "v_add_u32 %[w12], %[d], %[w12]\n\t"
        "v_mfma_f32_16x16x32_bf16 %[a13], v[204:207], v[252:255], %[a13]\n\t"
        "s_add_u32 %[i], %[i], 1\n\t"
        "s_bitcmp1_b32 36, %[i]\n\t"
        "v_mfma_f32_16x16x32_bf16 %[a12], v[204:207], v[248:251], %[a12]\n\t"
        "s_cselect_b32 %[d], %[dn], %[dp]\n\t"
        "s_cmp_lg_u32 %[i], 8\n\t"
        "v_mfma_f32_16x16x32_bf16 %[a11], v[204:207], v[244:247], %[a11]\n\t"
        "v_mfma_f32_16x16x32_bf16 %[a10], v[204:207], v[240:243], %[a10]\n\t"
        "s_waitcnt lgkmcnt(0)\n\t"
        "s_cbranch_scc0 4f\n\t"
        "s_barrier\n\t"
        "v_mfma_f32_16x16x32_bf16 %[a20], v[208:211], v[240:243], %[a20]\n\t"
        "ds_read_b128 v[224:227], %[x0] offset:0\n\t"
        "v_mfma_f32_16x16x32_bf16 %[a21], v[208:211], v[244:247], %[a21]\n\t"
        "ds_read_b128 v[228:231], %[x0] offset:2048\n\t"
        "v_mfma_f32_16x16x32_bf16 %[a22], v[208:211], v[248:251], %[a22]\n\t"
        "ds_read_b64_tr_b16 v[200:201], %[w00] offset:0\n\t"
        "v_mfma_f32_16x16x32_bf16 %[a23], v[208:211], v[252:255], %[a23]\n\t"
        "ds_read_b64_tr_b16 v[202:203], %[w00] offset:768\n\t"
        "v_mfma_f32_16x16x32_bf16 %[a33], v[212:215], v[252:255], %[a33]\n\t"
        "ds_read_b128 v[232:235], %[x0] offset:4096\n\t"
        "v_mfma_f32_16x16x32_bf16 %[a32], v[212:215], v[248:251], %[a32]\n\t"
        "ds_read_b128 v[236:239], %[x0] offset:6144\n\t"
        "v_mfma_f32_16x16x32_bf16 %[a31], v[212:215], v[244:247], %[a31]\n\t"
        "ds_read_b64_tr_b16 v[204:205], %[w00] offset:12288\n\t"
        "v_mfma_f32_16x16x32_bf16 %[a30], v[212:215], v[240:243], %[a30]\n\t"
        "ds_read_b64_tr_b16 v[206:207], %[w00] offset:13056\n\t"
        "v_mfma_f32_16x16x32_bf16 %[a40], v[216:219], v[240:243], %[a40]\n\t"
        "ds_read_b64_tr_b16 v[208:209], %[w01] offset:0\n\t"
        "v_mfma_f32_16x16x32_bf16 %[a41], v[216:219], v[244:247], %[a41]\n\t"
        "ds_read_b64_tr_b16 v[210:211], %[w01] offset:768\n\t"
        "v_mfma_f32_16x16x32_bf16 %[a42], v[216:219], v[248:251], %[a42]\n\t"
        "ds_read_b64_tr_b16 v[212:213], %[w01] offset:12288\n\t"
        "v_mfma_f32_16x16x32_bf16 %[a43], v[216:219], v[252:255], %[a43]\n\t"
        "ds_read_b64_tr_b16 v[214:215], %[w01] offset:13056\n\t"
        "v_mfma_f32_16x16x32_bf16 %[a53], v[220:223], v[252:255], %[a53]\n\t"
        "ds_read_b64_tr_b16 v[216:217], %[w02] offset:0\n\t"
        "v_mfma_f32_16x16x32_bf16 %[a52], v[220:223], v[248:251], %[a52]\n\t"
        "ds_read_b64_tr_b16 v[218:219], %[w02] offset:768\n\t"
        "v_mfma_f32_16x16x32_bf16 %[a51], v[220:223], v[244:247], %[a51]\n\t"
        "v_mfma_f32_16x16x32_bf16 %[a50], v[220:223], v[240:243], %[a50]\n\t"
        "s_waitcnt lgkmcnt(6)\n\t"
        "v_mfma_f32_16x16x32_bf16 %[a00], v[200:203], v[224:227], %[a00]\n\t"
        "ds_read_b64_tr_b16 v[220:221], %[w02] offset:12288\n\t"
        "v_mfma_f32_16x16x32_bf16 %[a01], v[200:203], v[228:231], %[a01]\n\t"
        "ds_read_b64_tr_b16 v[222:223], %[w02] offset:13056\n\t"
        "v_mfma_f32_16x16x32_bf16 %[a02], v[200:203], v[232:235], %[a02]\n\t"
        "v_add_u32 %[x0], %[d], %[x0]\n\t"
        "v_mfma_f32_16x16x32_bf16 %[a03], v[200:203], v[236:239], %[a03]\n\t"
        "v_add_u32 %[w00], %[d], %[w00]\n\t"
        "v_mfma_f32_16x16x32_bf16 %[a13], v[204:207], v[236:239], %[a13]\n\t"
        "v_add_u32 %[w01], %[d], %[w01]\n\t"
        "v_mfma_f32_16x16x32_bf16 %[a12], v[204:207], v[232:235], %[a12]\n\t"
        "v_add_u32 %[w02], %[d], %[w02]\n\t"
        "v_mfma_f32_16x16x32_bf16 %[a11], v[204:207], v[228:231], %[a11]\n\t"
        "v_smfmac_f32_16x16x64_bf16 v[196:199], %[sel0], v[200:207], %[sidx]\n\t"
        "v_mfma_f32_16x16x32_bf16 %[a10], v[204:207], v[224:227], %[a10]\n\t"
        "s_waitcnt lgkmcnt(0)\n\t"
        "v_mfma_f32_16x16x32_bf16 %[a20], v[208:211], v[224:227], %[a20]\n\t"
        "ds_read_b128 v[240:243], %[x1] offset:0\n\t"
        "v_mfma_f32_16x16x32_bf16 %[a21], v[208:211], v[228:231], %[a21]\n\t"
        "ds_read_b128 v[244:247], %[x1] offset:2048\n\t"
        "v_mfma_f32_16x16x32_bf16 %[a22], v[208:211], v[232:235], %[a22]\n\t"
        "ds_read_b64_tr_b16 v[200:201], %[w10] offset:0\n\t"
        "v_mfma_f32_16x16x32_bf16 %[a23], v[208:211], v[236:239], %[a23]\n\t"
        "ds_read_b64_tr_b16 v[202:203], %[w10] offset:768\n\t"
        "v_mfma_f32_16x16x32_bf16 %[a33], v[212:215], v[236:239], %[a33]\n\t"
        "ds_read_b128 v[248:251], %[x1] offset:4096\n\t"
        "v_mfma_f32_16x16x32_bf16 %[a32], v[212:215], v[232:235], %[a32]\n\t"
        "ds_read_b128 v[252:255], %[x1] offset:6144\n\t"
        "v_mfma_f32_16x16x32_bf16 %[a31], v[212:215], v[228:231], %[a31]\n\t"
        "ds_read_b64_tr_b16 v[204:205], %[w10] offset:12288\n\t"
        "v_mfma_f32_16x16x32_bf16 %[a30], v[212:215], v[224:227], %[a30]\n\t"
        "ds_read_b64_tr_b16 v[206:207], %[w10] offset:13056\n\t"
        "v_smfmac_f32_16x16x64_bf16 v[196:199], %[sel1], v[208:215], %[sidx]\n\t"
        "v_mfma_f32_16x16x32_bf16 %[a40], v[216:219], v[224:227], %[a40]\n\t"
        "ds_read_b64_tr_b16 v[208:209], %[w11] offset:0\n\t"
        "v_mfma_f32_16x16x32_bf16 %[a41], v[216:219], v[228:231], %[a41]\n\t"
        "ds_read_b64_tr_b16 v[210:211], %[w11] offset:768\n\t"
        "v_mfma_f32_16x16x32_bf16 %[a42], v[216:219], v[232:235], %[a42]\n\t"
        "ds_read_b64_tr_b16 v[212:213], %[w11] offset:12288\n\t"
        "v_mfma_f32_16x16x32_bf16 %[a43], v[216:219], v[236:239], %[a43]\n\t"
        "ds_read_b64_tr_b16 v[214:215], %[w11] offset:13056\n\t"
        "v_smfmac_f32_16x16x64_bf16 v[196:199], %[sel2], v[216:223], %[sidx]\n\t"
        "v_mfma_f32_16x16x32_bf16 %[a53], v[220:223], v[236:239], %[a53]\n\t"
        "ds_read_b64_tr_b16 v[216:217], %[w12] offset:0\n\t"
        "v_mfma_f32_16x16x32_bf16 %[a52], v[220:223], v[232:235], %[a52]\n\t"
        "ds_read_b64_tr_b16 v[218:219], %[w12] offset:768\n\t"
        "v_mfma_f32_16x16x32_bf16 %[a51], v[220:223], v[228:231], %[a51]\n\t"
        "v_add_u32 %[x1], %[d], %[x1]\n\t"
        "v_mfma_f32_16x16x32_bf16 %[a50], v[220:223], v[224:227], %[a50]\n\t"
        "v_add_u32 %[w10], %[d], %[w10]\n\t"
        "s_branch 1b\n\t"
        "4:\n\t"
        "v_mfma_f32_16x16x32_bf16 %[a20], v[208:211], v[240:243], %[a20]\n\t"
        "v_add_f32 v196, v196, v197\n\t"
        "v_add_f32 v198, v198, v199\n\t"
        "ds_read_b32 %[mn0], %[sa] offset:0\n\t"
        "v_mfma_f32_16x16x32_bf16 %[a21], v[208:211], v[244:247], %[a21]\n\t"
        "ds_read_b32 %[mn1], %[sa] offset:64\n\t"
        "ds_read_b32 %[mn2], %[sa] offset:128\n\t"
        "ds_read_b32 %[mn3], %[sa] offset:192\n\t"
        "v_mfma_f32_16x16x32_bf16 %[a22], v[208:211], v[248:251], %[a22]\n\t"
        "ds_write2_b32 %[ca], v196, v198 offset1:16\n\t"
        "v_mfma_f32_16x16x32_bf16 %[a23], v[208:211], v[252:255], %[a23]\n\t"
        "ds_read_b128 v[172:175], %[co] offset:0\n\t"
        "ds_read_b128 v[176:179], %[co] offset:64\n\t"
        "v_mfma_f32_16x16x32_bf16 %[a33], v[212:215], v[252:255], %[a33]\n\t"
        "ds_read_b128 v[180:183], %[co] offset:128\n\t"
        "ds_read_b128 v[184:187], %[co] offset:192\n\t"
        "v_mfma_f32_16x16x32_bf16 %[a32], v[212:215], v[248:251], %[a32]\n\t"
        "ds_read_b128 v[188:191], %[co] offset:256\n\t"
        "ds_read_b128 v[192:195], %[co] offset:320\n\t"
        "v_mfma_f32_16x16x32_bf16 %[a31], v[212:215], v[244:247], %[a31]\n\t"
        "s_waitcnt lgkmcnt(7)\n\t"
        "v_and_b32 %[sd0], 0xffff0000, %[mn0]\n\t"
        "v_lshlrev_b32 %[mn0], 16, %[mn0]\n\t"
        "v_mfma_f32_16x16x32_bf16 %[a30], v[212:215], v[240:243], %[a30]\n\t"
        "v_rcp_f32 %[sc0], %[sd0]\n\t"
        "v_and_b32 %[sd1], 0xffff0000, %[mn1]\n\t"
        "v_lshlrev_b32 %[mn1], 16, %[mn1]\n\t"
        "v_mfma_f32_16x16x32_bf16 %[a40], v[216:219], v[240:243], %[a40]\n\t"
        "v_rcp_f32 %[sc1], %[sd1]\n\t"
        "v_and_b32 %[sd2], 0xffff0000, %[mn2]\n\t"
        "v_lshlrev_b32 %[mn2], 16, %[mn2]\n\t"
        "v_mfma_f32_16x16x32_bf16 %[a41], v[216:219], v[244:247], %[a41]\n\t"
        "v_rcp_f32 %[sc2], %[sd2]\n\t"
        "v_and_b32 %[sd3], 0xffff0000, %[mn3]\n\t"
        "v_lshlrev_b32 %[mn3], 16, %[mn3]\n\t"
        "v_mfma_f32_16x16x32_bf16 %[a42], v[216:219], v[248:251], %[a42]\n\t"
        "v_rcp_f32 %[sc3], %[sd3]\n\t"
        "v_mfma_f32_16x16x32_bf16 %[a43], v[216:219], v[252:255], %[a43]\n\t"
        "v_mul_f32 %[sc0], 0xbfb8aa3b, %[sc0]\n\t"
        "ds_read_b32 v199, %[fp]\n\t"
        "ds_read_b128 v[224:227], %[cp] offset:0\n\t"
        "v_mfma_f32_16x16x32_bf16 %[a53], v[220:223], v[252:255], %[a53]\n\t"
        "v_mul_f32 %[sc1], 0xbfb8aa3b, %[sc1]\n\t"
        "ds_read_b128 v[228:231], %[cp] offset:64\n\t"
        "ds_read_b128 v[232:235], %[cp] offset:128\n\t"
        "v_mfma_f32_16x16x32_bf16 %[a52], v[220:223], v[248:251], %[a52]\n\t"
        "v_mul_f32 %[sc2], 0xbfb8aa3b, %[sc2]\n\t"
        "ds_read_b128 v[236:239], %[cp] offset:192\n\t"
        "ds_read_b128 v[200:203], %[cp] offset:256\n\t"
        "v_mfma_f32_16x16x32_bf16 %[a51], v[220:223], v[244:247], %[a51]\n\t"
        "v_mul_f32 %[sc3], 0xbfb8aa3b, %[sc3]\n\t"
        "ds_read_b128 v[204:207], %[cp] offset:320\n\t"
        "v_mfma_f32_16x16x32_bf16 %[a50], v[220:223], v[240:243], %[a50]\n\t"
        "5:\n\t"
        "s_waitcnt lgkmcnt(0)\n\t"
        "v_readfirstlane_b32 %[d], v199\n\t"
        "s_add_u32 %[i], %[i], 0x10000\n\t"
        "s_cbranch_scc1 6f\n\t"
        "s_cmp_lg_u32 %[d], 0\n\t"
        "s_cbranch_scc1 6f\n\t"
        "ds_read_b32 v199, %[fp]\n\t"
        "ds_read_b128 v[224:227], %[cp] offset:0\n\t"
        "ds_read_b128 v[228:231], %[cp] offset:64\n\t"
        "ds_read_b128 v[232:235], %[cp] offset:128\n\t"
        "ds_read_b128 v[236:239], %[cp] offset:192\n\t"
        "ds_read_b128 v[200:203], %[cp] offset:256\n\t"
        "ds_read_b128 v[204:207], %[cp] offset:320\n\t"
        "s_branch 5b\n\t"
        "6:\n\t"
        "v_pk_add_f32 %[cl0], v[172:173], v[224:225]\n\t"
        "v_pk_add_f32 %[ch0], v[174:175], v[226:227]\n\t"
        "v_pk_add_f32 %[cl1], v[176:177], v[228:229]\n\t"
        "v_pk_add_f32 %[ch1], v[178:179], v[230:231]\n\t"
        "v_pk_add_f32 %[cl2], v[180:181], v[232:233]\n\t"
        "v_pk_add_f32 %[ch2], v[182:183], v[234:235]\n\t"
        "v_pk_add_f32 %[cl3], v[184:185], v[236:237]\n\t"
        "v_pk_add_f32 %[ch3], v[186:187], v[238:239]\n\t"
        "v_pk_add_f32 %[cl4], v[188:189], v[200:201]\n\t"
        "v_pk_add_f32 %[ch4], v[190:191], v[202:203]\n\t"
        "v_pk_add_f32 %[cl5], v[192:193], v[204:205]\n\t"
        "v_pk_add_f32 %[ch5], v[194:195], v[206:207]\n\t"
        : AO(0), AO(1), AO(2), AO(3), AO(4), AO(5), G1IN, [i] "+s"(cnt), [d] "=&s"(dlt),
          SO(0), SO(1), SO(2), SO(3)
        : [dp] "s"(SLOT), [dn] "s"(0u - 2 * SLOT), [ca] "v"(L_CS + w * 512 + q * 128 + n * 4), [one] "v"(q == 3 ? 1.f : 0.f),
          [fp] "v"(L_CS + (w ^ 1) * 512 + 508), [co] "v"(L_CS + w * 512 + q * 16), [cp] "v"(L_CS + (w ^ 1) * 512 + q * 16), [sa] "v"(L_ST + (wm * 64 + n) * 4)
        : "memory", "scc",
          "v172", "v173", "v174", "v175", "v176", "v177", "v178", "v179", "v180", "v181", "v182", "v183", "v184", "v185", "v186", "v187",
          "v188", "v189", "v190", "v191", "v192", "v193", "v194", "v195", "v196", "v197", "v198", "v199", "v200", "v201", "v202", "v203",
          "v204", "v205", "v206", "v207", "v208", "v209", "v210", "v211", "v212", "v213", "v214", "v215", "v216", "v217", "v218", "v219",
          "v220", "v221", "v222", "v223", "v224", "v225", "v226", "v227", "v228", "v229", "v230", "v231", "v232", "v233", "v234", "v235",
          "v236", "v237", "v238", "v239", "v240", "v241", "v242", "v243", "v244", "v245", "v246", "v247", "v248", "v249", "v250", "v251",
          "v252", "v253", "v254", "v255");
      lf_post(LF_C + w, 2);
      const float nm[4] = {-mn[0], -mn[1], -mn[2], -mn[3]};
      f32x4 cv[6];
#pragma unroll
      for (int j = 0; j < 6; j++) cv[j] = (f32x4){cl[j].x, cl[j].y, ch[j].x, ch[j].y};
#pragma unroll
      for (int j = 0; j < 3; j++) {
        const int ftg = 6 * ct + 3 * wn + j;
#pragma unroll
        for (int tb = 0; tb < 2; tb++) {
          u32 d[2][2];
#pragma unroll
          for (int u = 0; u < 2; u++) {
            const int t = 2 * tb + u;
            const f32x4 g = __builtin_elementwise_fma(cv[2 * j], (f32x4)nm[t], a[2 * j][t]) * sc[t];
            const f32x4 e = {__builtin_amdgcn_exp2f(g.x), __builtin_amdgcn_exp2f(g.y), __builtin_amdgcn_exp2f(g.z), __builtin_amdgcn_exp2f(g.w)};
            const f32x4 dn = __builtin_elementwise_fma(e, (f32x4)sd[t], (f32x4)sd[t]);
            const f32x4 rc = {__builtin_amdgcn_rcpf(dn.x), __builtin_amdgcn_rcpf(dn.y), __builtin_amdgcn_rcpf(dn.z), __builtin_amdgcn_rcpf(dn.w)};
            const f32x4 hv = __builtin_elementwise_fma(cv[2 * j + 1], (f32x4)nm[t], a[2 * j + 1][t]) * rc;
            d[u][0] = pk(hv.x, hv.y); d[u][1] = pk(hv.z, hv.w);
          }
          const v2u s0 = __builtin_amdgcn_permlane16_swap(d[0][0], d[1][0], false, false), s1 = __builtin_amdgcn_permlane16_swap(d[0][1], d[1][1], false, false);
          st_sc1(wsr, (v4u){s0.x, s1.x, s0.y, s1.y}, lane * 16, ftg < NFT ? WS_H + ((4 * rt + 2 * wm + tb) * NFT + ftg) * 1024 : 0x80000000u);
        }
      }
      lf_post(LF_C + w, 3);
      lf_wait(LF_C + (lane & 3), 2);
      wd_q(10, 10 + WDF);
      flag_set<WDF, 8>(wsr, WS_FH + ((2 * rt + wm) * 32 + 2 * ct + wn) * 128, tag, lane);
    } else {
#pragma unroll 1
      for (int i = 0; i < 3; i++) bar();
#pragma unroll
      for (int i = 0; i < 3; i++) touch(64 * i + lane < 172 ? (u32)((rt * 4 + w) * 172 + 64 * i + lane) * 128 : 0x80000000u, rs_wd);
#pragma unroll 1
      for (int i = 3; i < 7; i++) bar();
      lf_post(LF_C + w, 3);
      wd_q(10, 10 + WDF);
    }

    const int tb = w & 1, kh = w >> 1;
    const int qq = (lane >> 2) & 3;
    const u32 db = (8 * h + qq) * 128 + 32 * (q & 1) + 8 * (lane & 3) + kh * 2048;
    u32 da[3][2];
#pragma unroll
    for (int i = 0; i < 3; i++) {
      da[i][0] = db + 64 * (qq >> 1) + 65536 * i; da[i][1] = db + 64 * (1 - (qq >> 1)) + 65536 * i;
      asm("" : "+v"(da[i][0]), "+v"(da[i][1]));
    }
    auto sloc = [](int i) -> u32 { return 4096u * (i < 10 ? i + 20 : i < 37 ? i - 10 : i - 7); };
    auto wfrag = [&](int i, int dt) { const u32 a = da[sloc(i) >> 16][dt] + (sloc(i) & 65535); return cat(lds_tr(a), lds_tr(a + 512)); };
    wd_q(10 + WDF, 30);
    xres = ald(xres_off, rs_x, 0);
    v4u hb[PF];
    const u32 hbase = WS_H + ((2 * tm + tb) * NFT + 43 * kh) * 1024;
    int ph = rot;
    flag_wait(rs_ws, WS_FH + (tm * 32 + 14 * kh + (lane < 15 ? lane : 0)) * 128, tag);
#pragma unroll
    for (int i = 0; i < PF; i++) { hb[i] = __builtin_amdgcn_raw_buffer_load_b128(wsr, lane * 16, hbase + ph * 1024, 16); ph = ph == 42 ? 0 : ph + 1; }
    asm volatile("" : "+v"(xres));
    fence_sched();
    lf_wait(LF_W + (lane & 3), 1);
    f32x16 o0 = {}, o1 = {};
    v4u wb[PL + 1][2];
#pragma unroll
    for (int i = 0; i < PL; i++) { wb[i][0] = wfrag(i, 0); wb[i][1] = wfrag(i, 1); }
#pragma unroll
    for (int i = 0; i < 43; i++) {
      if (i == 7 - PL) lf_post(LF_C + w, 4);
      if (i == 10 - PL) lf_wait(LF_C + (lane & 3), 4);
      if (i == ZG) lf_post(LF_C + w, 5);
      if (i == 24 - PL) lf_post(LF_C + w, 6);
      if (i == 30 - PL) lf_wait(LF_W + (lane & 3), 2);
      if (i == 37 - PL) lf_wait((h ? LF_W : LF_C) + (lane & 3), 6);
      if (i + PL < 43) { wb[(i + PL) % (PL + 1)][0] = wfrag(i + PL, 0); wb[(i + PL) % (PL + 1)][1] = wfrag(i + PL, 1); }
      o0 = mfma32(wb[i % (PL + 1)][0], hb[i % PF], o0);
      o1 = mfma32(wb[i % (PL + 1)][1], hb[i % PF], o1);
      if (i + PF < 43) { hb[i % PF] = __builtin_amdgcn_raw_buffer_load_b128(wsr, lane * 16, hbase + ph * 1024, 16); ph = ph == 42 ? 0 : ph + 1; }
      fence_sched();
    }
    {
      const u32 pa = L_P + kh * P_BUF + (32 * tb + l31) * P_PITCH + 16 * h;
#pragma unroll
      for (int g = 0; g < 4; g++) {
        lds_st<f32x4>(pa + 32 * g, (f32x4){o0[4 * g], o0[4 * g + 1], o0[4 * g + 2], o0[4 * g + 3]});
        lds_st<f32x4>(pa + 128 + 32 * g, (f32x4){o1[4 * g], o1[4 * g + 1], o1[4 * g + 2], o1[4 * g + 3]});
      }
    }
  } else {
    const int hw = w - 4;
    const u32 vd0 = (lane >> 3) * 1024 + 16 * ((lane & 7) ^ (4 * ((lane >> 4) & 1))), vd1 = vd0 + 7168;
    auto wd_step = [&](u32 loc, int step) {
      int ph = rot + step; ph = ph >= 43 ? ph - 43 : ph;
      const u32 s0 = ph * 16384 + tn * 128;
      dma4(loc, vd0, vd1, rs_wd, s0, s0 + 43 * 16384 - 2048);
    };
    v4u sv0, sv1, xv0, xv1, rv[8];
    auto gather = [&]() {
      sv0 = ald(lane * 16, rs_ws, WS_STATS + hw * 2048); sv1 = ald(lane * 16, rs_ws, WS_STATS + hw * 2048 + 1024);
      xv0 = ald(lane * 16, rs_ws, WS_XT + b * 8192 + hw * 2048); xv1 = ald(lane * 16, rs_ws, WS_XT + b * 8192 + hw * 2048 + 1024);
      xres = ald(xres_off, rs_x, 0);
    };
    if (w < 6) {
      __builtin_amdgcn_s_setprio(3);
      if (freecu) {
        const u32 li = (rt * 2 + hw) * 64 + lane;
        u32 e0, e1;
        asm volatile("buffer_load_dword %0, %2, %4, 0 offen sc1\n\tbuffer_load_dword %1, %3, %4, 0 offen sc1" : "=&v"(e0), "=&v"(e1)
                     : "v"(lane < 9 ? (lane ? WS_TSTAT - 64 + (li >> 6) * 512 + lane * 64 : WS_FX + li) : 0x80000000u),
                       "v"(li < 1024 ? WS_FH + li * 128 : WS_FZ - 65536 + li * 64), "s"(rs_ws) : "memory");
        scalars_ready();
        wait_vm<2>();
#pragma unroll 1
        for (int i = 0; i < 7; i++) bar_raw();
        asm volatile("s_waitcnt vmcnt(0)" : "+v"(e0), "+v"(e1));
      } else if (w == 4) {
        auto issue = [&](int c, int g0, int g1) {
          const u32 so = rt * 131072 + ((kc0 + c) & 7) * 128, lb = (c % 3) * SLOT;
#pragma unroll
          for (int g = g0; g < g1; g++) dma4(lb + g * 4096, vx0, vx1, rs_x, so + g * 32768, so + g * 32768 + 14336);
        };
        issue(1, 0, HXG);
        scalars_ready();
        wait_vm<4 * HXG>();
#pragma unroll 1
        for (int i = 0; i < 6; i++) { wait_vm<4 * HXG>(); bar_raw(); issue(i + 1, HXG, 4); issue(i + 2, 0, HXG); }
        wait_vm<4 * HXG>(); bar_raw(); issue(7, HXG, 4);
      } else {
        auto issue = [&](int c) {
          const u32 so = ((kc0 + c) & 7) * (64 * FF * 2) + ct * 192, lb = (c % 3) * SLOT + S_WG;
#pragma unroll
          for (int g = 0; g < 8; g++) dma3(lb + (g & 1) * 12288 + (g >> 1) * 3072, vo, (g & 1) ? rs_u : rs_g, so + (g >> 1) * (16 * FF * 2));
        };
        issue(1);
        scalars_ready();
        wait_vm<24>();
#pragma unroll 1
        for (int i = 0; i < 6; i++) { wait_vm<24>(); bar_raw(); issue(i + 2); }
        wait_vm<24>(); bar_raw();
      }
      wd_step(2 * SLOT + hw * 4096, hw);
      wait_vm<4>(); bar_raw();
      gather();
#pragma unroll
      for (int i = 1; i < 5; i++) wd_step(2 * SLOT + (2 * i + hw) * 4096, 2 * i + hw);
      __builtin_amdgcn_s_setprio(0);
      asm volatile("s_waitcnt vmcnt(%5)" : "+v"(sv0), "+v"(sv1), "+v"(xv0), "+v"(xv1), "+v"(xres) : "i"(16));
    } else {
      const int sw = w - 6;
      scalars_ready();
      if (tid < 396) lds_st<v4u>(lane < 8 ? L_FLAG + 16 * lane : L_CS + 512 * lane - 3600, (v4u){0u, 0u, 0u, 0u});
      asm volatile("s_waitcnt vmcnt(0)" : "+v"(xr0), "+v"(xr1), "+v"(xr2), "+v"(xr3));
      {
        const v4u xr[4] = {xr0, xr1, xr2, xr3};
        float s[4], qq[4];
#pragma unroll
        for (int t = 0; t < 4; t++) {
          s[t] = 0.f; qq[t] = 0.f;
#pragma unroll
          for (int j = 0; j < 4; j++) { const u32 v = xr[t][j]; s[t] = dot2(v, 0x3f803f80u, s[t]); qq[t] = dot2(v, v, qq[t]); }
        }
        const float mean = row_sum(fold16(fold32(s[0], s[2]), fold32(s[1], s[3]))) * (1.f / DM);
        const float sq = row_sum(fold16(fold32(qq[0], qq[2]), fold32(qq[1], qq[3]))) * (1.f / DM);
        const float sd = __builtin_sqrtf(rbf(rbf(__builtin_fmaxf(sq - mean * mean, 0.f)) + 1e-5f));
        if ((lane & 15) == 0) {
          const u32 t0 = rt * 128 + ct * 8 + 4 * sw;
          __builtin_amdgcn_raw_buffer_store_b32(pk(mean, sd), wsr, lane >> 2, WS_STATS + t0 * 4, 16);
          __builtin_amdgcn_raw_buffer_store_b64((v2u){pk(mean, sd), tag}, wsr, lane >> 1, WS_TSTAT + t0 * 8, 16);
        }
      }
      {
        const int tok = 64 * sw + 32 * (lane >> 5) + 4 * (lane & 7), pl = (lane >> 3) & 3;
        u32 ta[4][2], va = (pl * SEQ + tok) * 4 + WS_XT + ((2 * kc0 + (ct >> 3)) * 16 * SEQ + rt * 128) * 4;
#pragma unroll
        for (int j = 0; j < 4; j++)
#pragma unroll
          for (int i = 0; i < 2; i++) ta[j][i] = tok * 128 + 16 * ((4 * (ct >> 3) + j) ^ (((tok >> 1) + i) & 7)) + 4 * pl;
        asm volatile("" : "+v"(ta[0][0]), "+v"(ta[0][1]), "+v"(ta[1][0]), "+v"(ta[1][1]), "+v"(ta[2][0]), "+v"(ta[2][1]), "+v"(ta[3][0]), "+v"(ta[3][1]), "+v"(va));
        bar();
        v4u v[4];
#pragma unroll
        for (int j = 0; j < 4; j++)
#pragma unroll
          for (int i = 0; i < 4; i++) v[j][i] = lds_ld<u32>(ta[j][i >> 1] + i * 128);
        __builtin_amdgcn_s_sleep(4);
#pragma unroll
        for (int j = 0; j < 4; j++) __builtin_amdgcn_raw_buffer_store_b128(v[j], wsr, va, 4 * j * SEQ * 4, 16);
      }
      bar();
      flag_set<0>(wsr, WS_FX + (rt * 32 + ct * 2 + sw) * 4, tag, lane);
      bar();
      const u32 kp = L_KPART + 2 * sw * KPART;
      auto k1 = [&](int i) { return lds_ld<float>(kp + 4 * i) + lds_ld<float>(kp + KPART + 4 * i); };
      auto k4 = [&](int i) {
        const f32x4 p = lds_ld<f32x4>(kp + 4 * i), r = lds_ld<f32x4>(kp + KPART + 4 * i);
        float sx = p.x + r.x, sy = p.y + r.y, sz = p.z + r.z, sv = p.w + r.w;
        asm("" : "+v"(sx), "+v"(sy), "+v"(sz), "+v"(sv));
        return (f32x4){sx, sy, sz, sv};
      };
      {
        const u32 s0 = (lane & 1) ? 0x01000100u : 0x01000302u, s1 = (lane & 1) ? 0x01000302u : 0x03020302u;
#pragma unroll
        for (int it = 0; it < 2; it++) {
          float ka = k1(639 - 64 * it - lane), kc = k1(640 - 64 * it - lane);
          asm("" : "+v"(ka), "+v"(kc));
          const u32 p = pk(ka, kc), aa = perm(p, p, 0x01000100u), cc = perm(p, p, 0x03020302u);
          rv[it] = (v4u){cc, perm(p, p, s0), aa, aa};
          rv[4 + it] = (v4u){cc, perm(p, p, s1), aa, aa};
        }
        asm volatile("" : "+v"(rv[0]), "+v"(rv[1]), "+v"(rv[4]), "+v"(rv[5]));
        bar();
        float ka = k1(510 - 2 * lane), kb = k1(511 - 2 * lane), kc = k1(512 - 2 * lane);
        asm("" : "+v"(ka), "+v"(kb), "+v"(kc));
        const u32 aa = pk(ka, ka), bb = pk(kb, kb);
        rv[2] = (v4u){pk(kc, kb), bb, aa, aa};
        rv[6] = (v4u){pk(kc, kc), bb, pk(kb, ka), aa};
      }
      asm volatile("" : "+v"(rv[2]), "+v"(rv[6]));
      bar();
      {
        const bool hi = lane < 32;
        const int ia = hi ? 380 - 4 * lane : 504 - 8 * lane;
        f32x4 a = k4(ia), c = k4(ia + 4);
        const float k0 = lane == 63 ? 0.f : k1(lane < 31 ? ia : imax(503 - 8 * lane, 0));
        if (lane == 63) a.x += sw ? bfhi(dk2) : bflo(dk2);
        const float kv[9] = {k0, a.x, hi ? a.x : a.y, hi ? a.y : a.z, hi ? a.z : a.w, hi ? a.z : c.x, hi ? a.z : c.y, hi ? a.w : c.z, hi ? c.x : c.w};
        rv[3] = (v4u){pk(kv[7], kv[6]), pk(kv[5], kv[4]), pk(kv[3], kv[2]), pk(kv[1], kv[0])};
        rv[7] = (v4u){pk(kv[8], kv[7]), pk(kv[6], kv[5]), pk(kv[4], kv[3]), pk(kv[2], kv[1])};
      }
      asm volatile("" : "+v"(rv[3]), "+v"(rv[7]));
      bar();
      if (freecu) __builtin_amdgcn_s_sleep(64);
      v4u fl, fm;
      v2u st;
#define FXLD(c) asm volatile("buffer_load_dwordx4 %0, %3, %5, %6 offen sc1\n\tbuffer_load_dwordx4 %1, %3, %5, %6 offen offset:1024 sc1\n\t" \
  "buffer_load_dwordx2 %2, %4, %5, %7 offen sc1" : c(fl), c(fm), c(st) \
  : "v"(lane * 16), "v"(lane * 8), "s"(rs_ws), "s"(WS_FX), "s"(WS_TSTAT + rt * 1024 + sw * 512) : "memory")
      FXLD("=&v");
      bar();
      __builtin_amdgcn_s_sleep(3);
#pragma unroll 1
      for (u32 i = 0; i < SPIN_CAP; i++) {
        asm volatile("s_waitcnt vmcnt(0)" : "+v"(fl), "+v"(fm), "+v"(st));
        const v4u df = (fl ^ tag) | (fm ^ tag);
        if (__builtin_amdgcn_ballot_w64((df.x | df.y | df.z | df.w | (st.y ^ tag)) != 0) == 0) break;
        __builtin_amdgcn_s_sleep(8);
        FXLD("+v");
      }
      gather();
      lds_st<u32>(L_ST + (64 * sw + lane) * 4, st.x);
      if (b == 0 && tid == 448) __builtin_amdgcn_raw_buffer_store_b32(tag, wsr, 0, WS_EPOCH, 16);
      bar();
      asm volatile("s_waitcnt vmcnt(0)" : "+v"(sv0), "+v"(sv1), "+v"(xv0), "+v"(xv1), "+v"(xres));
    }
    v2u pa[2], pc[2];
#pragma unroll
    for (int i = 0; i < 2; i++) {
      const v4u sv = i ? sv1 : sv0, xv = i ? xv1 : xv0;
      float a[4], c[4];
#pragma unroll
      for (int j = 0; j < 4; j++) {
        const float r = __builtin_amdgcn_rcpf(bfhi(sv[j])), m = -bflo(sv[j]) * r;
        a[j] = __builtin_fmaf(bflo(xv[j]), r, m); c[j] = __builtin_fmaf(bfhi(xv[j]), r, m);
      }
      asm("" : "+v"(a[0]), "+v"(a[1]), "+v"(a[2]), "+v"(a[3]), "+v"(c[0]), "+v"(c[1]), "+v"(c[2]), "+v"(c[3]));
      pa[i] = (v2u){pk(a[0], a[1]), pk(a[2], a[3])}; pc[i] = (v2u){pk(c[0], c[1]), pk(c[2], c[3])};
    }
    u32 ub = L_U + 448 + (512 * hw + 4 * lane) * 2, rb = L_R + (hw & 1) * R_CH + 16 * lane;
    asm volatile("" : "+v"(pa[0]), "+v"(pa[1]), "+v"(pc[0]), "+v"(pc[1]), "+v"(ub), "+v"(rb));
    lf_wait(LF_C + (lane & 3), 2);
    if (w >= 6) {
#pragma unroll
      for (int i = 0; i < 8; i++) lds_st<v4u>(rb + 1024 * (i & 3) + R_COPY * (i >> 2), rv[i]);
    } else {
      const u32 za = lane < 28 ? L_U + hw * U_CH + lane * 16 : lane < 56 ? L_U + hw * U_CH + U_COPY + (lane - 28) * 16
                                                                         : L_R + hw * R_CH + 4096 + (lane & 3) * 16 + ((lane >> 2) & 1) * R_COPY;
      lds_st<v4u>(za, (v4u){0u, 0u, 0u, 0u});
      if (lane == 0) lds_st<u32>(L_U + hw * U_CH + 448, 0u);
    }
#pragma unroll
    for (int i = 0; i < 2; i++) {
      const u32 u = ub + 512 * i;
      lds_st<v2u>(u + U_COPY, pa[i]); lds_st<u32>(u + 4, pa[i].x); lds_st<u32>(u + 8, pa[i].y);
      lds_st<v2u>(u + U_CH + U_COPY, pc[i]); lds_st<u32>(u + U_CH + 4, pc[i].x); lds_st<u32>(u + U_CH + 8, pc[i].y);
    }
    lf_set(LF_S + hw, 1);
    lf_wait(LF_S + (lane & 3), 1);

    const int cc = hw & 1, y = hw >> 1;
    const int n = lane & 15, q = lane >> 4, odd = lane & 1;
    u32 ra = L_R + cc * R_CH + odd * R_COPY + (2044 - 4 * (n >> 1) + 8 * q) * 2 - 7 * 512 - 256 * y;
    u32 ua = L_U + cc * U_CH + odd * U_COPY + 448 + 64 * (n >> 1) + 16 * q - 256 * y;
    const u32 xr = L_R + cc * R_CH, xu = L_U + cc * U_CH, qq = lane & 3, m = (lane >> 2) & 7;
    const u32 xo = (4 * y + (lane >> 5)) * 1024 + qq * 256 + ((m * 32) ^ (qq * 64) ^ ((m >> 2) * 16));
    u32 cnt = 0, cap = 0, ft, fv;
    wait_vm<0>();
    lf_set(LF_W + hw, 1);
    asm volatile(
        "ds_read_b64 v[220:221], %[ra] offset:0\n\t"
        "ds_read_b64 v[222:223], %[ra] offset:8\n\t"
        "ds_read_b64 v[216:217], %[ra] offset:512\n\t"
        "ds_read_b64 v[218:219], %[ra] offset:520\n\t"
        "ds_read_b64 v[212:213], %[ra] offset:1024\n\t"
        "ds_read_b64 v[214:215], %[ra] offset:1032\n\t"
        "ds_read_b64 v[208:209], %[ra] offset:1536\n\t"
        "ds_read_b64 v[210:211], %[ra] offset:1544\n\t"
        "ds_read_b64 v[204:205], %[ra] offset:2048\n\t"
        "ds_read_b64 v[206:207], %[ra] offset:2056\n\t"
        "ds_read_b64 v[200:201], %[ra] offset:2560\n\t"
        "ds_read_b64 v[202:203], %[ra] offset:2568\n\t"
        "ds_read_b64 v[196:197], %[ra] offset:3072\n\t"
        "ds_read_b64 v[198:199], %[ra] offset:3080\n\t"
        "v_mov_b64 v[160:161], 0\n\t"
        "v_mov_b64 v[162:163], 0\n\t"
        "v_mov_b64 v[164:165], 0\n\t"
        "v_mov_b64 v[166:167], 0\n\t"
        "v_mov_b64 v[168:169], 0\n\t"
        "v_mov_b64 v[170:171], 0\n\t"
        "v_mov_b64 v[172:173], 0\n\t"
        "v_mov_b64 v[174:175], 0\n\t"
        "v_mov_b64 v[176:177], 0\n\t"
        "v_mov_b64 v[178:179], 0\n\t"
        "v_mov_b64 v[180:181], 0\n\t"
        "v_mov_b64 v[182:183], 0\n\t"
        "v_mov_b64 v[184:185], 0\n\t"
        "v_mov_b64 v[186:187], 0\n\t"
        "v_mov_b64 v[188:189], 0\n\t"
        "v_mov_b64 v[190:191], 0\n\t"
        "s_waitcnt lgkmcnt(4)\n\t"
        "ds_read_b128 v[224:227], %[ua] offset:0\n\t"
        "ds_read_b128 v[228:231], %[ua] offset:512\n\t"
        "ds_read_b128 v[232:235], %[ua] offset:1024\n\t"
        "ds_read_b128 v[236:239], %[ua] offset:1536\n\t"
        "ds_read_b128 v[240:243], %[ua] offset:2048\n\t"
        "ds_read_b128 v[244:247], %[ua] offset:2560\n\t"
        "ds_read_b128 v[248:251], %[ua] offset:3072\n\t"
        "ds_read_b128 v[252:255], %[ua] offset:3584\n\t"
        "ds_read_b64 v[192:193], %[ra] offset:3584\n\t"
        "ds_read_b64 v[194:195], %[ra] offset:3592\n\t"
        "ds_read_b32 %[f], %[fg]\n\t"
        "2:\n\t"
        "s_waitcnt lgkmcnt(0)\n\t"
        "v_readfirstlane_b32 %[t], %[f]\n\t"
        "s_add_u32 %[c], %[c], 0x1000\n\t"
        "s_cbranch_scc1 3f\n\t"
        "s_cmp_ge_u32 %[t], %[gl]\n\t"
        "s_cbranch_scc1 3f\n\t"
        "s_sleep 1\n\t"
        "ds_read_b32 %[f], %[fg]\n\t"
        "s_branch 2b\n\t"
        "3:\n\t"
        "s_setprio 3\n\t"
        "1:\n\t"
        "v_subrev_u32 %[ra], 64, %[ra]\n\t"
        "v_subrev_u32 %[ua], 64, %[ua]\n\t"
        "s_waitcnt lgkmcnt(9)\n\t"
        "v_mfma_f32_16x16x32_bf16 v[188:191], v[220:223], v[224:227], v[188:191]\n\t"
        "v_mfma_f32_16x16x32_bf16 v[184:187], v[216:219], v[224:227], v[184:187]\n\t"
        "ds_read_b64 v[220:221], %[ra] offset:0\n\t"
        "s_waitcnt lgkmcnt(9)\n\t"
        "v_mfma_f32_16x16x32_bf16 v[188:191], v[216:219], v[228:231], v[188:191]\n\t"
        "ds_read_b64 v[222:223], %[ra] offset:8\n\t"
        "v_mfma_f32_16x16x32_bf16 v[180:183], v[212:215], v[224:227], v[180:183]\n\t"
        "ds_read_b64 v[216:217], %[ra] offset:512\n\t"
        "v_mfma_f32_16x16x32_bf16 v[184:187], v[212:215], v[228:231], v[184:187]\n\t"
        "ds_read_b64 v[218:219], %[ra] offset:520\n\t"
        "s_waitcnt lgkmcnt(11)\n\t"
        "v_mfma_f32_16x16x32_bf16 v[188:191], v[212:215], v[232:235], v[188:191]\n\t"
        "v_mfma_f32_16x16x32_bf16 v[176:179], v[208:211], v[224:227], v[176:179]\n\t"
        "ds_read_b64 v[212:213], %[ra] offset:1024\n\t"
        "v_mfma_f32_16x16x32_bf16 v[180:183], v[208:211], v[228:231], v[180:183]\n\t"
        "ds_read_b64 v[214:215], %[ra] offset:1032\n\t"
        "v_mfma_f32_16x16x32_bf16 v[184:187], v[208:211], v[232:235], v[184:187]\n\t"
        "s_waitcnt lgkmcnt(6)\n\t"
        "v_mfma_f32_16x16x32_bf16 v[188:191], v[208:211], v[236:239], v[188:191]\n\t"
        "v_mfma_f32_16x16x32_bf16 v[172:175], v[204:207], v[224:227], v[172:175]\n\t"
        "ds_read_b64 v[208:209], %[ra] offset:1536\n\t"
        "v_mfma_f32_16x16x32_bf16 v[176:179], v[204:207], v[228:231], v[176:179]\n\t"
        "ds_read_b64 v[210:211], %[ra] offset:1544\n\t"
        "v_mfma_f32_16x16x32_bf16 v[180:183], v[204:207], v[232:235], v[180:183]\n\t"
        "v_mfma_f32_16x16x32_bf16 v[184:187], v[204:207], v[236:239], v[184:187]\n\t"
        "v_mfma_f32_16x16x32_bf16 v[188:191], v[204:207], v[240:243], v[188:191]\n\t"
        "v_mfma_f32_16x16x32_bf16 v[168:171], v[200:203], v[224:227], v[168:171]\n\t"
        "ds_read_b64 v[204:205], %[ra] offset:2048\n\t"
        "v_mfma_f32_16x16x32_bf16 v[172:175], v[200:203], v[228:231], v[172:175]\n\t"
        "ds_read_b64 v[206:207], %[ra] offset:2056\n\t"
        "v_mfma_f32_16x16x32_bf16 v[176:179], v[200:203], v[232:235], v[176:179]\n\t"
        "v_mfma_f32_16x16x32_bf16 v[180:183], v[200:203], v[236:239], v[180:183]\n\t"
        "v_mfma_f32_16x16x32_bf16 v[184:187], v[200:203], v[240:243], v[184:187]\n\t"
        "v_mfma_f32_16x16x32_bf16 v[188:191], v[200:203], v[244:247], v[188:191]\n\t"
        "v_mfma_f32_16x16x32_bf16 v[164:167], v[196:199], v[224:227], v[164:167]\n\t"
        "ds_read_b64 v[200:201], %[ra] offset:2560\n\t"
        "v_mfma_f32_16x16x32_bf16 v[168:171], v[196:199], v[228:231], v[168:171]\n\t"
        "ds_read_b64 v[202:203], %[ra] offset:2568\n\t"
        "v_mfma_f32_16x16x32_bf16 v[172:175], v[196:199], v[232:235], v[172:175]\n\t"
        "v_mfma_f32_16x16x32_bf16 v[176:179], v[196:199], v[236:239], v[176:179]\n\t"
        "v_mfma_f32_16x16x32_bf16 v[180:183], v[196:199], v[240:243], v[180:183]\n\t"
        "v_mfma_f32_16x16x32_bf16 v[184:187], v[196:199], v[244:247], v[184:187]\n\t"
        "s_add_u32 %[i], %[i], 1\n\t"
        "v_mfma_f32_16x16x32_bf16 v[188:191], v[196:199], v[248:251], v[188:191]\n\t"
        "s_cmp_lt_u32 %[i], 3\n\t"
        "v_mfma_f32_16x16x32_bf16 v[160:163], v[192:195], v[224:227], v[160:163]\n\t"
        "ds_read_b64 v[196:197], %[ra] offset:3072\n\t"
        "ds_read_b128 v[224:227], %[ua] offset:0\n\t"
        "v_mfma_f32_16x16x32_bf16 v[164:167], v[192:195], v[228:231], v[164:167]\n\t"
        "ds_read_b64 v[198:199], %[ra] offset:3080\n\t"
        "ds_read_b128 v[228:231], %[ua] offset:512\n\t"
        "v_mfma_f32_16x16x32_bf16 v[168:171], v[192:195], v[232:235], v[168:171]\n\t"
        "ds_read_b128 v[232:235], %[ua] offset:1024\n\t"
        "v_mfma_f32_16x16x32_bf16 v[172:175], v[192:195], v[236:239], v[172:175]\n\t"
        "ds_read_b128 v[236:239], %[ua] offset:1536\n\t"
        "v_mfma_f32_16x16x32_bf16 v[176:179], v[192:195], v[240:243], v[176:179]\n\t"
        "ds_read_b128 v[240:243], %[ua] offset:2048\n\t"
        "v_mfma_f32_16x16x32_bf16 v[180:183], v[192:195], v[244:247], v[180:183]\n\t"
        "ds_read_b128 v[244:247], %[ua] offset:2560\n\t"
        "v_mfma_f32_16x16x32_bf16 v[184:187], v[192:195], v[248:251], v[184:187]\n\t"
        "ds_read_b128 v[248:251], %[ua] offset:3072\n\t"
        "v_mfma_f32_16x16x32_bf16 v[188:191], v[192:195], v[252:255], v[188:191]\n\t"
        "ds_read_b128 v[252:255], %[ua] offset:3584\n\t"
        "ds_read_b64 v[192:193], %[ra] offset:3584\n\t"
        "ds_read_b64 v[194:195], %[ra] offset:3592\n\t"
        "s_cbranch_scc1 1b\n\t"
        "v_mov_b32 %[f], 3\n\t"
        "ds_write_b32 %[fa], %[f]\n\t"
        "s_waitcnt lgkmcnt(10)\n\t"
        "v_mfma_f32_16x16x32_bf16 v[188:191], v[220:223], v[224:227], v[188:191]\n\t"
        "v_mfma_f32_16x16x32_bf16 v[184:187], v[216:219], v[224:227], v[184:187]\n\t"
        "s_waitcnt lgkmcnt(9)\n\t"
        "v_mfma_f32_16x16x32_bf16 v[188:191], v[216:219], v[228:231], v[188:191]\n\t"
        "v_mfma_f32_16x16x32_bf16 v[180:183], v[212:215], v[224:227], v[180:183]\n\t"
        "v_mfma_f32_16x16x32_bf16 v[184:187], v[212:215], v[228:231], v[184:187]\n\t"
        "s_waitcnt lgkmcnt(8)\n\t"
        "v_mfma_f32_16x16x32_bf16 v[188:191], v[212:215], v[232:235], v[188:191]\n\t"
        "v_mfma_f32_16x16x32_bf16 v[176:179], v[208:211], v[224:227], v[176:179]\n\t"
        "v_mfma_f32_16x16x32_bf16 v[180:183], v[208:211], v[228:231], v[180:183]\n\t"
        "v_mfma_f32_16x16x32_bf16 v[184:187], v[208:211], v[232:235], v[184:187]\n\t"
        "s_waitcnt lgkmcnt(1)\n\t"
        "v_mfma_f32_16x16x32_bf16 v[188:191], v[208:211], v[236:239], v[188:191]\n\t"
        "v_mfma_f32_16x16x32_bf16 v[188:191], v[204:207], v[240:243], v[188:191]\n\t"
        "v_mfma_f32_16x16x32_bf16 v[184:187], v[204:207], v[236:239], v[184:187]\n\t"
        "v_mfma_f32_16x16x32_bf16 v[188:191], v[200:203], v[244:247], v[188:191]\n\t"
        "v_mfma_f32_16x16x32_bf16 v[184:187], v[200:203], v[240:243], v[184:187]\n\t"
        "ds_read_b32 %[f], %[fp]\n\t"
        "v_mfma_f32_16x16x32_bf16 v[188:191], v[196:199], v[248:251], v[188:191]\n\t"
        "v_mfma_f32_16x16x32_bf16 v[184:187], v[196:199], v[244:247], v[184:187]\n\t"
        "v_mfma_f32_16x16x32_bf16 v[188:191], v[192:195], v[252:255], v[188:191]\n\t"
        "v_mfma_f32_16x16x32_bf16 v[184:187], v[192:195], v[248:251], v[184:187]\n\t"
        "v_mfma_f32_16x16x32_bf16 v[180:183], v[204:207], v[232:235], v[180:183]\n\t"
        "v_mfma_f32_16x16x32_bf16 v[176:179], v[204:207], v[228:231], v[176:179]\n\t"
        "v_mfma_f32_16x16x32_bf16 v[180:183], v[200:203], v[236:239], v[180:183]\n\t"
        "v_mfma_f32_16x16x32_bf16 v[176:179], v[200:203], v[232:235], v[176:179]\n\t"
        "4:\n\t"
        "s_waitcnt lgkmcnt(0)\n\t"
        "v_readfirstlane_b32 %[t], %[f]\n\t"
        "s_add_u32 %[c], %[c], 0x1000\n\t"
        "s_cbranch_scc1 5f\n\t"
        "s_cmp_ge_u32 %[t], 3\n\t"
        "s_cbranch_scc1 5f\n\t"
        "ds_read_b32 %[f], %[fp]\n\t"
        "s_branch 4b\n\t"
        "5:\n\t"
        "v_mfma_f32_16x16x32_bf16 v[180:183], v[196:199], v[240:243], v[180:183]\n\t"
        "ds_write_b128 %[wa], v[188:191] offset:7168\n\t"
        "v_mfma_f32_16x16x32_bf16 v[176:179], v[196:199], v[236:239], v[176:179]\n\t"
        "v_mfma_f32_16x16x32_bf16 v[180:183], v[192:195], v[244:247], v[180:183]\n\t"
        "ds_write_b128 %[wa], v[184:187] offset:6144\n\t"
        "v_mfma_f32_16x16x32_bf16 v[176:179], v[192:195], v[240:243], v[176:179]\n\t"
        "v_mfma_f32_16x16x32_bf16 v[172:175], v[204:207], v[224:227], v[172:175]\n\t"
        "v_mfma_f32_16x16x32_bf16 v[168:171], v[200:203], v[224:227], v[168:171]\n\t"
        "v_mfma_f32_16x16x32_bf16 v[164:167], v[196:199], v[224:227], v[164:167]\n\t"
        "v_mfma_f32_16x16x32_bf16 v[160:163], v[192:195], v[224:227], v[160:163]\n\t"
        "v_mfma_f32_16x16x32_bf16 v[172:175], v[200:203], v[228:231], v[172:175]\n\t"
        "v_mfma_f32_16x16x32_bf16 v[168:171], v[196:199], v[228:231], v[168:171]\n\t"
        "v_mfma_f32_16x16x32_bf16 v[164:167], v[192:195], v[228:231], v[164:167]\n\t"
        "ds_write_b128 %[wa], v[180:183] offset:5120\n\t"
        "v_mfma_f32_16x16x32_bf16 v[172:175], v[196:199], v[232:235], v[172:175]\n\t"
        "ds_write_b128 %[wa], v[176:179] offset:4096\n\t"
        "v_mfma_f32_16x16x32_bf16 v[168:171], v[192:195], v[232:235], v[168:171]\n\t"
        "v_mfma_f32_16x16x32_bf16 v[172:175], v[192:195], v[236:239], v[172:175]\n\t"
        "v_mov_b32 %[f], 5\n\t"
        "ds_write_b128 %[wa], v[160:163] offset:0\n\t"
        "s_nop 1\n\t"
        "ds_write_b128 %[wa], v[164:167] offset:1024\n\t"
        "s_nop 1\n\t"
        "ds_write_b128 %[wa], v[168:171] offset:2048\n\t"
        "s_nop 0\n\t"
        "ds_write_b128 %[wa], v[172:175] offset:3072\n\t"
        "ds_write_b32 %[fa], %[f]\n\t"
        "ds_read_b128 v[192:195], %[o0] offset:0\n\t"
        "ds_read_b128 v[196:199], %[o1] offset:0\n\t"
        "ds_read_b128 v[200:203], %[o0] offset:2048\n\t"
        "ds_read_b128 v[204:207], %[o1] offset:2048\n\t"
        "ds_read_b32 %[f], %[fp]\n\t"
        "ds_read_b128 v[224:227], %[p0] offset:0\n\t"
        "ds_read_b128 v[228:231], %[p1] offset:0\n\t"
        "ds_read_b128 v[232:235], %[p0] offset:2048\n\t"
        "ds_read_b128 v[236:239], %[p1] offset:2048\n\t"
        "6:\n\t"
        "s_waitcnt lgkmcnt(0)\n\t"
        "v_readfirstlane_b32 %[t], %[f]\n\t"
        "s_add_u32 %[c], %[c], 0x1000\n\t"
        "s_cbranch_scc1 7f\n\t"
        "s_cmp_ge_u32 %[t], 5\n\t"
        "s_cbranch_scc1 7f\n\t"
        "ds_read_b32 %[f], %[fp]\n\t"
        "ds_read_b128 v[224:227], %[p0] offset:0\n\t"
        "ds_read_b128 v[228:231], %[p1] offset:0\n\t"
        "ds_read_b128 v[232:235], %[p0] offset:2048\n\t"
        "ds_read_b128 v[236:239], %[p1] offset:2048\n\t"
        "s_branch 6b\n\t"
        "7:\n\t"
        "v_mov_b32 %[f], 6\n\t"
        "ds_write_b32 %[fa], %[f]\n\t"
        "v_pk_add_f32 v[192:193], v[192:193], v[224:225]\n\t"
        "v_pk_add_f32 v[194:195], v[194:195], v[226:227]\n\t"
        "v_pk_add_f32 v[196:197], v[196:197], v[228:229]\n\t"
        "v_pk_add_f32 v[198:199], v[198:199], v[230:231]\n\t"
        "v_pk_add_f32 v[200:201], v[200:201], v[232:233]\n\t"
        "v_pk_add_f32 v[202:203], v[202:203], v[234:235]\n\t"
        "v_pk_add_f32 v[204:205], v[204:205], v[236:237]\n\t"
        "v_pk_add_f32 v[206:207], v[206:207], v[238:239]\n\t"
        "v_cvt_pk_bf16_f32 v160, v192, v193\n\t"
        "v_cvt_pk_bf16_f32 v161, v196, v197\n\t"
        "v_cvt_pk_bf16_f32 v162, v194, v195\n\t"
        "v_cvt_pk_bf16_f32 v163, v198, v199\n\t"
        "buffer_store_dwordx4 v[160:163], %[zo], %[rs], 0 offen offset:0 sc1\n\t"
        "v_cvt_pk_bf16_f32 v164, v200, v201\n\t"
        "v_cvt_pk_bf16_f32 v165, v204, v205\n\t"
        "v_cvt_pk_bf16_f32 v166, v202, v203\n\t"
        "v_cvt_pk_bf16_f32 v167, v206, v207\n\t"
        "buffer_store_dwordx4 v[164:167], %[zo], %[rs], 0 offen offset:1024 sc1\n\t"
        "s_nop 1\n\t"
        : [ra] "+v"(ra), [ua] "+v"(ua), [i] "+s"(cnt), [c] "+s"(cap), [t] "=&s"(ft), [f] "=&v"(fv)
        : [fa] "v"(L_FLAG + 4 * (LF_X + hw)), [fp] "v"(L_FLAG + 4 * (LF_X + (hw ^ 2))), [fg] "v"(L_FLAG + 4 * (LF_C + hw)), [gl] "i"(3),
          [wa] "v"((y ? xu : xr) + q * 256 + ((n * 16) ^ (q * 64) ^ ((n >> 3) * 16))), [o0] "v"((y ? xu : xr) + xo), [o1] "v"((y ? xu : xr) + (xo ^ 16)),
          [p0] "v"((y ? xr : xu) + xo), [p1] "v"((y ? xr : xu) + (xo ^ 16)), [zo] "v"(WS_ZT + ((2 * b + cc) * SEQ + 1024 * y) * 2 + lane * 16), [rs] "s"(rs_ws)
        : "memory", "scc",
          "v160", "v161", "v162", "v163", "v164", "v165", "v166", "v167", "v168", "v169", "v170", "v171", "v172", "v173", "v174", "v175",
          "v176", "v177", "v178", "v179", "v180", "v181", "v182", "v183", "v184", "v185", "v186", "v187", "v188", "v189", "v190", "v191",
          "v192", "v193", "v194", "v195", "v196", "v197", "v198", "v199", "v200", "v201", "v202", "v203", "v204", "v205", "v206", "v207",
          "v208", "v209", "v210", "v211", "v212", "v213", "v214", "v215", "v216", "v217", "v218", "v219", "v220", "v221", "v222", "v223",
          "v224", "v225", "v226", "v227", "v228", "v229", "v230", "v231", "v232", "v233", "v234", "v235", "v236", "v237", "v238", "v239",
          "v240", "v241", "v242", "v243", "v244", "v245", "v246", "v247", "v248", "v249", "v250", "v251", "v252", "v253", "v254", "v255");
    lf_wait(LF_X + (lane & 3), 6);
    wd_q(37, 43);
    flag_set<6, 4>(wsr, WS_FZ + (y * DM + 2 * b + cc) * 64, tag, lane);
    lf_wait(LF_C + (lane & 3), 4);
    wd_q(30, 37);
    wait_vm<0>();
    lf_set(LF_W + hw, 6);
    lf_wait(LF_C + (lane & 3), 5);
    flag_wait(rs_ws, WS_FZ + ((tm >> 4) * DM + 64 * tn + 16 * hw + n) * 64, tag);
    v4u zv[2];
#pragma unroll
    for (int i = 0; i < 2; i++) zv[i] = ld_sc1(wsr, (lane >> 3) * (SEQ * 2) + (lane & 7) * 16, WS_ZT + ((64 * tn + 16 * hw + 8 * i) * SEQ + 64 * tm) * 2);
#pragma unroll
    for (int i = 0; i < 2; i++) lds_st<v4u>(L_Z + hw * 2048 + i * 1024 + lane * 16, zv[i]);
    lf_wait(LF_C + (lane & 3), 6);
    asm volatile("" ::: "memory");
    const u32 za = L_Z + hw * 2048 + (n >> 2) * 128 + q * 32 + (lane & 3) * 8;
#pragma unroll
    for (int i = 0; i < 2; i++) lds_st<v4u>(L_ZT + lane * Z_PITCH + hw * 32 + i * 16, cat(lds_tr(za + i * 1024), lds_tr(za + i * 1024 + 512)));
    __builtin_amdgcn_s_setprio(2);
  }

  f32x4 x0 = {bflo(xres.x), bfhi(xres.x), bflo(xres.y), bfhi(xres.y)}, x1 = {bflo(xres.z), bfhi(xres.z), bflo(xres.w), bfhi(xres.w)};
  const u32 ln = lane_id();
  u32 pa = L_P + (8 * w + (ln >> 3)) * P_PITCH + (ln & 7) * 32, pz = L_ZT + (8 * w + (ln >> 3)) * Z_PITCH + (ln & 7) * 16;
  asm volatile("" : "+v"(x0), "+v"(x1), "+v"(pa), "+v"(pz));
  bar();
  {
    const v4u z = lds_ld<v4u>(pz);
    x0 += (f32x4){bflo(z.x), bfhi(z.x), bflo(z.y), bfhi(z.y)};
    x1 += (f32x4){bflo(z.z), bfhi(z.z), bflo(z.w), bfhi(z.w)};
    x0 += lds_ld<f32x4>(pa) + lds_ld<f32x4>(pa + P_BUF);
    x1 += lds_ld<f32x4>(pa + 16) + lds_ld<f32x4>(pa + P_BUF + 16);
    const v4u o = {pk(x0.x, x0.y), pk(x0.z, x0.w), pk(x1.x, x1.y), pk(x1.z, x1.w)};
    __builtin_amdgcn_raw_buffer_store_b128(o, __builtin_amdgcn_make_buffer_rsrc((void*)kb[2], 0, SEQ * DM * 2, 0x00020000), xres_off, 0, 16);
  }
}
}

extern "C" void kernel_launch(void* const* d_in, const int* in_sizes, int n_in, void* d_out, int out_size, void* d_ws,
                              size_t ws_size, hipStream_t stream) {
  Params p;
  p.x = (const u16*)d_in[0]; p.lar = (const u16*)d_in[1]; p.aim = (const u16*)d_in[2]; p.cre = (const u16*)d_in[3];
  p.cim = (const u16*)d_in[4]; p.ldt = (const u16*)d_in[5]; p.dsk = (const u16*)d_in[6];
  p.wg = (const u16*)d_in[8]; p.wu = (const u16*)d_in[9]; p.wd = (const u16*)d_in[10];
  p.out = (u16*)d_out; p.ws = (char*)d_ws;
  fused_kernel<<<256, 512, 0, stream>>>(p);
}
```
